```python
import torch

def _softmax(x, axis=-1):
    return torch.softmax(x, dim=axis)
def _gelu(x):
    return torch.nn.functional.gelu(x, approximate="tanh")
def _silu(x):
    return torch.nn.functional.silu(x)
def _sigmoid(x):
    return torch.sigmoid(x)
def _layernorm(x, axis=-1, eps=1e-5):
    m = x.mean(dim=axis, keepdim=True)
    v = x.var(dim=axis, keepdim=True, unbiased=False)
    return (x - m) / torch.sqrt(v + eps)
def _scatter_add(tgt, idx, src, axis=0):
    return tgt.clone().index_add_(axis, idx, src)
def _segment_sum(src, seg, n):
    out = torch.zeros((n,) + src.shape[1:], dtype=src.dtype)
    return out.index_add_(0, seg, src)
def _pad(x, pw):
    flat = tuple(v for pair in reversed(pw) for v in pair)
    return torch.nn.functional.pad(x, flat)

kernel_name = 'standalone__gqa_softmax__near_ridge'

def setup_inputs():

    _w = (lambda a: (lambda t: t.to(torch.bfloat16) if t.is_floating_point() else t)(torch.as_tensor(a)))
    B, Hq, Hkv, Sq, Sk, D = 8, 16, 8, 144, 1024, 256
    return {
        "q": _w((lambda *s: torch.randn(*s, dtype=torch.bfloat16) * 0.8)(B, Hq, Sq, D)),
        "k": _w((lambda *s: torch.randn(*s, dtype=torch.bfloat16) * 0.8)(B, Hkv, Sk, D)),
        "v": _w((lambda *s: torch.randn(*s, dtype=torch.bfloat16) * 0.05)(B, Hkv, Sk, D)),
    }

def reference(q, k, v):
    B, Hq, Sq, D = q.shape
    Hkv = k.shape[1]
    rep = Hq // Hkv
    k = (lambda x, n, axis: torch.repeat_interleave(x, n, dim=axis))(k, rep, axis=1)
    v = (lambda x, n, axis: torch.repeat_interleave(x, n, dim=axis))(v, rep, axis=1)
    scale = D ** -0.5
    scores = torch.einsum("bhqd,bhkd->bhqk", q, k) * scale
    p = _softmax(scores, axis=-1)
    return torch.einsum("bhqk,bhkd->bhqd", p, v)
```

```cpp
#include <hip/hip_runtime.h>
#include <hip/hip_bf16.h>

typedef __bf16 bf16x8 __attribute__((ext_vector_type(8)));
typedef float f32x4 __attribute__((ext_vector_type(4)));
typedef short s16x4 __attribute__((ext_vector_type(4)));
typedef unsigned u32x2 __attribute__((ext_vector_type(2)));
typedef unsigned u32x4 __attribute__((ext_vector_type(4)));
#define LDS_AS __attribute__((address_space(3)))

namespace {

constexpr int SLOT = 32768;
constexpr int P_OFF = 4 * SLOT;
constexpr int PBUF = 10240;
constexpr int MAX_OFF = P_OFF + 2 * PBUF;
constexpr int SUM_OFF = MAX_OFF + 1280;
constexpr int LDS_BYTES = SUM_OFF + 1280;
constexpr int Q_OFF = 3 * SLOT;

__device__ __forceinline__ unsigned cvtpk(float lo, float hi) {
  typedef float f32x2 __attribute__((ext_vector_type(2)));
  typedef __bf16 bf16x2 __attribute__((ext_vector_type(2)));
  return __builtin_bit_cast(unsigned, __builtin_convertvector(f32x2{lo, hi}, bf16x2));
}
template <int N> __device__ __forceinline__ void waitvm() { asm volatile("s_waitcnt vmcnt(%0)" ::"i"(N) : "memory"); }
__device__ __forceinline__ void wg_barrier() {
  asm volatile("s_waitcnt lgkmcnt(0)\n\ts_barrier" ::: "memory");
}
template <int OFF>
__device__ __forceinline__ void dma(__amdgpu_buffer_rsrc_t srd, unsigned voff, unsigned soff, unsigned m0v) {
  asm volatile("s_mov_b32 m0, %0\n\ts_nop 0\n\tbuffer_load_dwordx4 %1, %2, %3 offen offset:%4 lds"
               ::"s"(m0v), "v"(voff), "s"(srd), "s"(soff), "i"(OFF) : "memory");
}
__device__ __forceinline__ void dma_rowblock(__amdgpu_buffer_rsrc_t srd, unsigned voff, unsigned soff, unsigned ldsdst) {
  dma<0>(srd, voff, soff, ldsdst);
  dma<128>(srd, voff, soff, ldsdst + 896);
  dma<256>(srd, voff, soff, ldsdst + 2 * 896);
  dma<384>(srd, voff, soff, ldsdst + 3 * 896);
}
__device__ __forceinline__ float xg_max(float x) {
  auto s = __builtin_amdgcn_permlane16_swap(__float_as_uint(x), __float_as_uint(x), false, false);
  x = fmaxf(__uint_as_float(s[0]), __uint_as_float(s[1]));
  auto t = __builtin_amdgcn_permlane32_swap(__float_as_uint(x), __float_as_uint(x), false, false);
  return fmaxf(__uint_as_float(t[0]), __uint_as_float(t[1]));
}
__device__ __forceinline__ float xg_sum(float x) {
  auto s = __builtin_amdgcn_permlane16_swap(__float_as_uint(x), __float_as_uint(x), false, false);
  x = __uint_as_float(s[0]) + __uint_as_float(s[1]);
  auto t = __builtin_amdgcn_permlane32_swap(__float_as_uint(x), __float_as_uint(x), false, false);
  return __uint_as_float(t[0]) + __uint_as_float(t[1]);
}

struct Ctx {
  __amdgpu_buffer_rsrc_t srdK, srdV;
  unsigned voff;
  unsigned ldsw;
  int c;
};

__device__ __forceinline__ void issue_tile(const Ctx& cx, int T) {
  const int t = T & 15;
  const unsigned soff = (unsigned)(((t & ~3) | ((t + cx.c) & 3)) * SLOT);
  dma_rowblock(T < 16 ? cx.srdK : cx.srdV, cx.voff, soff, (unsigned)((T & 3) * SLOT) + cx.ldsw);
}

template <int T> __device__ __forceinline__ void step_sync(const Ctx& cx) {
  if constexpr (T == 1 || T == 30) waitvm<4>();
  else if constexpr (T == 31) waitvm<0>();
  else waitvm<8>();
  wg_barrier();
  if constexpr (T == 1) issue_tile(cx, 3);
  if constexpr (T >= 1 && T + 3 < 32) issue_tile(cx, T + 3);
}

template <int NT, int RT0>
__device__ __forceinline__ void body(LDS_AS char* lds, const Ctx& cx, int wave, int lane, __hip_bfloat16* __restrict__ outp) {
  const int pair = wave & 3;
  const int r = lane & 15, g = lane >> 4;
  constexpr float CEXP = 0.0625f * 1.4426950408889634f;

  step_sync<0>(cx);
  bf16x8 qf[NT][8];
  {
#pragma unroll
    for (int i = 0; i < NT; ++i) {
      const int rt = RT0 + i;
      const int rr = (rt == 4) ? (r & 7) : r;
      const int base = Q_OFF + (2 * rt + (rr >> 3)) * 4096 + (rr & 7) * 128;
#pragma unroll
      for (int ks = 0; ks < 8; ++ks)
        qf[i][ks] = *(LDS_AS bf16x8*)(lds + base + (ks >> 1) * 1024 + ((((ks & 1) * 4 + g) ^ (rr & 6)) * 16));
    }
  }
  const int kb0 = (2 * pair + (r >> 3)) * 4096 + (r & 7) * 128 + ((g ^ (r & 6)) * 16);
  const int kb1 = (2 * pair + (r >> 3)) * 4096 + (r & 7) * 128 + (((4 + g) ^ (r & 6)) * 16);

  u32x2 S[16][NT];
  float mx[NT];
#pragma unroll
  for (int i = 0; i < NT; ++i) mx[i] = -INFINITY;

  auto qk_step = [&](auto TC) {
    constexpr int T = decltype(TC)::value;
    if constexpr (T > 0) step_sync<T>(cx);
    f32x4 acc[NT];
#pragma unroll
    for (int i = 0; i < NT; ++i) acc[i] = f32x4{0.f, 0.f, 0.f, 0.f};
#pragma unroll
    for (int ks = 0; ks < 8; ++ks) {
      const bf16x8 kf = *(LDS_AS bf16x8*)(lds + (T & 3) * SLOT + ((ks & 1) ? kb1 : kb0) + (ks >> 1) * 1024);
#pragma unroll
      for (int i = 0; i < NT; ++i) acc[i] = __builtin_amdgcn_mfma_f32_16x16x32_bf16(kf, qf[i][ks], acc[i], 0, 0, 0);
    }
#pragma unroll
    for (int i = 0; i < NT; ++i) {
      mx[i] = fmaxf(fmaxf(mx[i], acc[i][0]), acc[i][1]);
      mx[i] = fmaxf(fmaxf(mx[i], acc[i][2]), acc[i][3]);
      S[T][i] = u32x2{cvtpk(acc[i][0], acc[i][1]), cvtpk(acc[i][2], acc[i][3])};
      asm volatile("" : "+v"(S[T][i]), "+v"(mx[i]));
    }
  };
  [&]<int... I>(std::integer_sequence<int, I...>) { (qk_step(std::integral_constant<int, I>{}), ...); }(std::make_integer_sequence<int, 16>{});

#pragma unroll
  for (int i = 0; i < NT; ++i) {
    float m = xg_max(mx[i]);
    if (g == 0) *(LDS_AS float*)(lds + MAX_OFF + (pair * 80 + (RT0 + i) * 16 + r) * 4) = m;
  }
  wg_barrier();
  float mc[NT], ls[NT];
#pragma unroll
  for (int i = 0; i < NT; ++i) {
    const int o = MAX_OFF + ((RT0 + i) * 16 + r) * 4;
    float m = fmaxf(fmaxf(*(LDS_AS float*)(lds + o), *(LDS_AS float*)(lds + o + 320)),
                    fmaxf(*(LDS_AS float*)(lds + o + 640), *(LDS_AS float*)(lds + o + 960)));
    m = __uint_as_float(cvtpk(m, m) << 16);
    mc[i] = -m * CEXP;
    ls[i] = 0.f;
  }
  const int pw = P_OFF + (pair >> 1) * 5120 + RT0 * 1024 + lane * 16 + (pair & 1) * 8;
  auto make_p = [&](auto TC) {
    constexpr int t = decltype(TC)::value;
#pragma unroll
    for (int i = 0; i < NT; ++i) {
      const unsigned a = S[t][i][0], b = S[t][i][1];
      const float e0 = __builtin_amdgcn_exp2f(__builtin_fmaf(__uint_as_float(a << 16), CEXP, mc[i]));
      const float e1 = __builtin_amdgcn_exp2f(__builtin_fmaf(__uint_as_float(a & 0xffff0000u), CEXP, mc[i]));
      const float e2 = __builtin_amdgcn_exp2f(__builtin_fmaf(__uint_as_float(b << 16), CEXP, mc[i]));
      const float e3 = __builtin_amdgcn_exp2f(__builtin_fmaf(__uint_as_float(b & 0xffff0000u), CEXP, mc[i]));
      ls[i] += (e0 + e1) + (e2 + e3);
      *(LDS_AS u32x2*)(lds + pw + (t & 1) * PBUF + i * 1024) = u32x2{cvtpk(e0, e1), cvtpk(e2, e3)};
    }
    if constexpr (t == 15) {
#pragma unroll
      for (int i = 0; i < NT; ++i) {
        float s = xg_sum(ls[i]);
        if (g == 0) *(LDS_AS float*)(lds + SUM_OFF + (pair * 80 + (RT0 + i) * 16 + r) * 4) = s;
      }
    }
  };
  make_p(std::integral_constant<int, 0>{});

  const int q4 = (lane >> 2) & 3, p4 = lane & 3;
  const int r7 = 4 * (g & 1) + q4;
  const int vrow = (g >> 1) * 4096 + (wave >> 1) * 1024 + r7 * 128 + (p4 & 1) * 8;
  const int vb0 = vrow + (((4 * (wave & 1) + (p4 >> 1)) ^ (r7 & 6)) * 16);
  const int vb1 = vrow + (((4 * (wave & 1) + 2 + (p4 >> 1)) ^ (r7 & 6)) * 16);
  f32x4 O[5][2];
#pragma unroll
  for (int i = 0; i < 5; ++i) { O[i][0] = f32x4{0.f, 0.f, 0.f, 0.f}; O[i][1] = f32x4{0.f, 0.f, 0.f, 0.f}; }

  auto pv_step = [&](auto TC) {
    constexpr int t = decltype(TC)::value;
    step_sync<16 + t>(cx);
#pragma unroll
    for (int j = 0; j < 2; ++j) {
      const int vo = (t & 3) * SLOT + j * 16384;
      const s16x4 a0 = __builtin_amdgcn_ds_read_tr16_b64_v4i16((LDS_AS s16x4*)(lds + vo + vb0));
      const s16x4 a1 = __builtin_amdgcn_ds_read_tr16_b64_v4i16((LDS_AS s16x4*)(lds + vo + vb0 + 8192));
      const s16x4 b0 = __builtin_amdgcn_ds_read_tr16_b64_v4i16((LDS_AS s16x4*)(lds + vo + vb1));
      const s16x4 b1 = __builtin_amdgcn_ds_read_tr16_b64_v4i16((LDS_AS s16x4*)(lds + vo + vb1 + 8192));
      const bf16x8 vf0 = __builtin_bit_cast(bf16x8, __builtin_shufflevector(a0, a1, 0, 1, 2, 3, 4, 5, 6, 7));
      const bf16x8 vf1 = __builtin_bit_cast(bf16x8, __builtin_shufflevector(b0, b1, 0, 1, 2, 3, 4, 5, 6, 7));
#pragma unroll
      for (int i = 0; i < 5; ++i) {
        const bf16x8 pf = *(LDS_AS bf16x8*)(lds + P_OFF + (t & 1) * PBUF + j * 5120 + i * 1024 + lane * 16);
        O[i][0] = __builtin_amdgcn_mfma_f32_16x16x32_bf16(vf0, pf, O[i][0], 0, 0, 0);
        O[i][1] = __builtin_amdgcn_mfma_f32_16x16x32_bf16(vf1, pf, O[i][1], 0, 0, 0);
      }
    }
    if constexpr (t < 15) make_p(std::integral_constant<int, t + 1>{});
  };
  [&]<int... I>(std::integer_sequence<int, I...>) { (pv_step(std::integral_constant<int, I>{}), ...); }(std::make_integer_sequence<int, 16>{});

  __hip_bfloat16* op = outp + r * 256 + wave * 32 + (g & 1) * 16 + (g >> 1) * 8;
#pragma unroll
  for (int i = 0; i < 5; ++i) {
    const int o = SUM_OFF + (i * 16 + r) * 4;
    const float s = (*(LDS_AS float*)(lds + o) + *(LDS_AS float*)(lds + o + 320)) +
                    (*(LDS_AS float*)(lds + o + 640) + *(LDS_AS float*)(lds + o + 960));
    const float inv = 1.0f / s;
    const unsigned a0 = cvtpk(O[i][0][0] * inv, O[i][0][1] * inv), a1 = cvtpk(O[i][0][2] * inv, O[i][0][3] * inv);
    const unsigned b0 = cvtpk(O[i][1][0] * inv, O[i][1][1] * inv), b1 = cvtpk(O[i][1][2] * inv, O[i][1][3] * inv);
    auto s0 = __builtin_amdgcn_permlane16_swap(a0, b0, false, false);
    auto s1 = __builtin_amdgcn_permlane16_swap(a1, b1, false, false);
    const u32x4 val = u32x4{s0[0], s1[0], s0[1], s1[1]};
    if (i < 4 || r < 8) *(u32x4*)(op + i * 16 * 256) = val;
  }
}

__global__ __launch_bounds__(512, 2) void gqa_attn_kernel(const __hip_bfloat16* __restrict__ q, const __hip_bfloat16* __restrict__ k,
                                                          const __hip_bfloat16* __restrict__ v, __hip_bfloat16* __restrict__ out) {
  __shared__ __attribute__((aligned(16))) char smem[LDS_BYTES];
  LDS_AS char* lds = (LDS_AS char*)smem;
  const int bid = blockIdx.x;
  const int idx = bid >> 3;
  const int grp = (bid & 7) * 8 + (idx >> 2);
  const int c = idx & 3;
  const int wave = __builtin_amdgcn_readfirstlane(threadIdx.x >> 6);
  const int lane = threadIdx.x & 63;

  Ctx cx;
  cx.srdK = __builtin_amdgcn_make_buffer_rsrc((void*)(k + (size_t)grp * 1024 * 256), (short)0, 1024 * 512, 0x00020000);
  cx.srdV = __builtin_amdgcn_make_buffer_rsrc((void*)(v + (size_t)grp * 1024 * 256), (short)0, 1024 * 512, 0x00020000);
  const size_t qrow0 = (size_t)grp * 288 + c * 72;
  __amdgpu_buffer_rsrc_t srdQ = __builtin_amdgcn_make_buffer_rsrc((void*)(q + qrow0 * 256), (short)0, 72 * 512, 0x00020000);
  const unsigned ldsbase = (unsigned)(unsigned long long)lds;
  const unsigned lrow = (unsigned)(lane >> 3);
  const unsigned lsw = (unsigned)(((lane & 7) ^ (lrow & 6)) * 16) + lrow * 512;
  cx.voff = lsw + (unsigned)wave * 4096u;
  cx.ldsw = ldsbase + (unsigned)wave * 4096u;
  cx.c = c;

  asm volatile("s_nop 4" ::: "memory");
  dma_rowblock(srdQ, cx.voff, 0u, cx.ldsw + Q_OFF);
  if (wave < 4) dma<0>(srdQ, lsw, 8u * 4096u + (unsigned)wave * 128u, ldsbase + Q_OFF + 8 * 4096 + (unsigned)wave * 1024u);
  issue_tile(cx, 0);
  issue_tile(cx, 1);
  issue_tile(cx, 2);

  __hip_bfloat16* outp = out + qrow0 * 256;
  if (wave < 4) body<3, 0>(lds, cx, wave, lane, outp);
  else body<2, 3>(lds, cx, wave, lane, outp);
}

}

extern "C" void kernel_launch(void* const* d_in, const int* in_sizes, int n_in, void* d_out, int out_size, void* d_ws,
                              size_t ws_size, hipStream_t stream) {
  const __hip_bfloat16* q = (const __hip_bfloat16*)d_in[0];
  const __hip_bfloat16* k = (const __hip_bfloat16*)d_in[1];
  const __hip_bfloat16* v = (const __hip_bfloat16*)d_in[2];
  gqa_attn_kernel<<<256, 512, 0, stream>>>(q, k, v, (__hip_bfloat16*)d_out);
}
```

```cpp
#include <hip/hip_runtime.h>
#include <hip/hip_bf16.h>
#include <utility>

typedef __bf16 bf16x8 __attribute__((ext_vector_type(8)));
typedef float f32x4 __attribute__((ext_vector_type(4)));
typedef short s16x4 __attribute__((ext_vector_type(4)));
typedef unsigned u32x2 __attribute__((ext_vector_type(2)));
typedef unsigned u32x4 __attribute__((ext_vector_type(4)));
#define LDS_AS __attribute__((address_space(3)))

#define ROT 0
#define LDPOL " nt"
#define ONT 1
namespace {

constexpr int SLOT = 32768;
constexpr int PBUF = 10240;
constexpr int P_OFF = 0;
constexpr int MAX_OFF = 2 * PBUF;
constexpr int SUM_OFF = MAX_OFF + 1280;
constexpr int RING = 24576;
constexpr int Q_OFF = RING + 3 * SLOT;
constexpr int LDS_BYTES = Q_OFF + 36864;
#define SB() __builtin_amdgcn_sched_barrier(0)

__device__ __forceinline__ unsigned cvtpk(float lo, float hi) {
  typedef float f32x2 __attribute__((ext_vector_type(2)));
  typedef __bf16 bf16x2 __attribute__((ext_vector_type(2)));
  return __builtin_bit_cast(unsigned, __builtin_convertvector(f32x2{lo, hi}, bf16x2));
}
template <int N> __device__ __forceinline__ void waitvm() { asm volatile("s_waitcnt vmcnt(%0)" ::"i"(N) : "memory"); }
__device__ __forceinline__ void wg_barrier() {
  asm volatile("s_waitcnt lgkmcnt(0)\n\ts_barrier" ::: "memory");
}
template <int OFF>
__device__ __forceinline__ void dma(__amdgpu_buffer_rsrc_t srd, unsigned voff, unsigned soff, unsigned m0v) {
  asm volatile("s_mov_b32 m0, %0\n\ts_nop 0\n\tbuffer_load_dwordx4 %1, %2, %3 offen offset:%4" LDPOL " lds"
               ::"s"(m0v), "v"(voff), "s"(srd), "s"(soff), "i"(OFF) : "memory");
}
__device__ __forceinline__ void dma_rowblock(__amdgpu_buffer_rsrc_t srd, unsigned voff, unsigned soff, unsigned ldsdst) {
  dma<0>(srd, voff, soff, ldsdst);
  dma<128>(srd, voff, soff, ldsdst + 896);
  dma<256>(srd, voff, soff, ldsdst + 2 * 896);
  dma<384>(srd, voff, soff, ldsdst + 3 * 896);
}
__device__ __forceinline__ float xg_max(float x) {
  auto s = __builtin_amdgcn_permlane16_swap(__float_as_uint(x), __float_as_uint(x), false, false);
  x = fmaxf(__uint_as_float(s[0]), __uint_as_float(s[1]));
  auto t = __builtin_amdgcn_permlane32_swap(__float_as_uint(x), __float_as_uint(x), false, false);
  return fmaxf(__uint_as_float(t[0]), __uint_as_float(t[1]));
}
__device__ __forceinline__ float xg_sum(float x) {
  auto s = __builtin_amdgcn_permlane16_swap(__float_as_uint(x), __float_as_uint(x), false, false);
  x = __uint_as_float(s[0]) + __uint_as_float(s[1]);
  auto t = __builtin_amdgcn_permlane32_swap(__float_as_uint(x), __float_as_uint(x), false, false);
  return __uint_as_float(t[0]) + __uint_as_float(t[1]);
}

#define STAMP(i)
struct Ctx {
  __amdgpu_buffer_rsrc_t srdK, srdV;
  unsigned voff;
  unsigned ldsw;
  int c;
};

__device__ __forceinline__ unsigned tile_soff(const Ctx& cx, int T) {
  const int t = T & 15;
  if (ROT == 0 || (ROT == 2 && t < 4)) return (unsigned)(t * SLOT);
  return (unsigned)(((t & ~3) | ((t + cx.c) & 3)) * SLOT);
}
template <int T, int PIECE> __device__ __forceinline__ void issue_piece(const Ctx& cx) {
  if constexpr (T < 32)
    dma<PIECE * 128>(T < 16 ? cx.srdK : cx.srdV, cx.voff, tile_soff(cx, T), (unsigned)(RING + (T & 3) * SLOT + PIECE * 896) + cx.ldsw);
}
template <int T> __device__ __forceinline__ void issue_tile(const Ctx& cx) {
  issue_piece<T, 0>(cx); issue_piece<T, 1>(cx); issue_piece<T, 2>(cx); issue_piece<T, 3>(cx);
}
template <int T, int LG> __device__ __forceinline__ void step_sync(const Ctx& cx) {
  SB();
  constexpr int VM = (T >= 30) ? 0 : 4;
  asm volatile("s_waitcnt vmcnt(%0) lgkmcnt(%1)\n\ts_barrier" ::"i"(VM), "i"(LG) : "memory");
  if constexpr ((T & 3) == 0) STAMP(1 + T / 4);
  if constexpr (T == 31) STAMP(9);
  SB();
}
__device__ __forceinline__ int opaque(int x) { asm volatile("" : "+v"(x)); return x; }
template <typename F, int... I> __device__ __forceinline__ void unroll_seq(F&& f, std::integer_sequence<int, I...>) {
  (f(std::integral_constant<int, I>{}), ...);
}

template <int NT, int RT0>
__device__ __forceinline__ void body(LDS_AS char* lds, const Ctx& cx, int wave, int lane, __hip_bfloat16* __restrict__ outp) {
  const int pair = wave & 3;
  const int r = lane & 15, g = lane >> 4;
  constexpr float CEXP = 0.0625f * 1.4426950408889634f;
  constexpr int PD = 3;

  const int kbase = RING + (2 * pair + (r >> 3)) * 4096 + (r & 7) * 128;
  int kb[2][2];
  kb[0][0] = opaque(kbase + ((g ^ (r & 6)) * 16));
  kb[1][0] = opaque(kbase + (((4 + g) ^ (r & 6)) * 16));
  kb[0][1] = opaque(kb[0][0] + 2 * SLOT);
  kb[1][1] = opaque(kb[1][0] + 2 * SLOT);
  auto kread = [&](int T, int ks) {
    return *(LDS_AS bf16x8*)(lds + kb[ks & 1][(T >> 1) & 1] + (T & 1) * SLOT + (ks >> 1) * 1024);
  };

  asm volatile("s_waitcnt vmcnt(8)\n\ts_barrier" ::: "memory");
  bf16x8 qf[NT][8];
  bf16x8 kf[8];
#pragma unroll
  for (int ks = 0; ks < PD; ++ks) kf[ks] = kread(0, ks);
#pragma unroll
  for (int ks = 0; ks < 8; ++ks) {
#pragma unroll
    for (int i = 0; i < NT; ++i) {
      const int rt = RT0 + i;
      const int rr = (rt == 4) ? (r & 7) : r;
      const int base = Q_OFF + (2 * rt + (rr >> 3)) * 4096 + (rr & 7) * 128;
      qf[i][ks] = *(LDS_AS bf16x8*)(lds + base + (ks >> 1) * 1024 + ((((ks & 1) * 4 + g) ^ (rr & 6)) * 16));
    }
  }

  u32x2 S[16][NT];
  float mx[NT];
#pragma unroll
  for (int i = 0; i < NT; ++i) mx[i] = -INFINITY;

  const int q4 = (lane >> 2) & 3, p4 = lane & 3;
  const int r7 = 4 * (g & 1) + q4;
  const int vrow = RING + (g >> 1) * 4096 + (wave >> 1) * 1024 + r7 * 128 + (p4 & 1) * 8;

  auto qk_step = [&](auto TC) {
    constexpr int T = decltype(TC)::value;
    if constexpr (T > 0) step_sync<T, 15>(cx);
    f32x4 acc[NT];
#pragma unroll
    for (int ks = 0; ks < 8; ++ks) {
      if constexpr (T == 0) { if (ks == 8 - PD) { step_sync<0, 0>(cx); issue_tile<3>(cx); SB(); } }
      if (ks + PD < 8) kf[ks + PD] = kread(T, ks + PD);
      else if (T < 15) kf[ks + PD - 8] = kread(T + 1, ks + PD - 8);
#pragma unroll
      for (int i = 0; i < NT; ++i)
        acc[i] = __builtin_amdgcn_mfma_f32_16x16x32_bf16(kf[ks], qf[i][ks], ks == 0 ? f32x4{0.f, 0.f, 0.f, 0.f} : acc[i], 0, 0, 0);
      SB();
      if constexpr (T >= 1) {
        constexpr int O = (NT == 3) ? 1 : 0;
        if (ks == O) issue_piece<T + 3, 0>(cx);
        if (ks == O + 2) issue_piece<T + 3, 1>(cx);
        if (ks == O + 4) issue_piece<T + 3, 2>(cx);
        if (ks == O + 6) issue_piece<T + 3, 3>(cx);
        SB();
      }
    }
#pragma unroll
    for (int i = 0; i < NT; ++i) {
      mx[i] = fmaxf(fmaxf(mx[i], acc[i][0]), acc[i][1]);
      mx[i] = fmaxf(fmaxf(mx[i], acc[i][2]), acc[i][3]);
      S[T][i] = u32x2{cvtpk(acc[i][0], acc[i][1]), cvtpk(acc[i][2], acc[i][3])};
      asm volatile("" : "+v"(S[T][i]), "+v"(mx[i]));
    }
  };
  unroll_seq(qk_step, std::make_integer_sequence<int, 16>{});

#pragma unroll
  for (int i = 0; i < NT; ++i) {
    float m = xg_max(mx[i]);
    if (g == 0) *(LDS_AS float*)(lds + MAX_OFF + (pair * 80 + (RT0 + i) * 16 + r) * 4) = m;
  }
  int vb[2][2];
  vb[0][0] = opaque(vrow + (((4 * (wave & 1) + (p4 >> 1)) ^ (r7 & 6)) * 16));
  vb[1][0] = opaque(vrow + (((4 * (wave & 1) + 2 + (p4 >> 1)) ^ (r7 & 6)) * 16));
  vb[0][1] = opaque(vb[0][0] + 2 * SLOT);
  vb[1][1] = opaque(vb[1][0] + 2 * SLOT);
  auto vread = [&](int t, int j, int dt) {
    const int o = vb[dt][(t >> 1) & 1] + (t & 1) * SLOT + j * 16384;
    const s16x4 a0 = __builtin_amdgcn_ds_read_tr16_b64_v4i16((LDS_AS s16x4*)(lds + o));
    const s16x4 a1 = __builtin_amdgcn_ds_read_tr16_b64_v4i16((LDS_AS s16x4*)(lds + o + 8192));
    return __builtin_bit_cast(bf16x8, __builtin_shufflevector(a0, a1, 0, 1, 2, 3, 4, 5, 6, 7));
  };
  wg_barrier();
  float mc[NT], ls[NT];
#pragma unroll
  for (int i = 0; i < NT; ++i) {
    const int o = MAX_OFF + ((RT0 + i) * 16 + r) * 4;
    float m = fmaxf(fmaxf(*(LDS_AS float*)(lds + o), *(LDS_AS float*)(lds + o + 320)),
                    fmaxf(*(LDS_AS float*)(lds + o + 640), *(LDS_AS float*)(lds + o + 960)));
    m = __uint_as_float(cvtpk(m, m) << 16);
    mc[i] = -m * CEXP;
    ls[i] = 0.f;
  }
  bf16x8 vf[2][2];
  vf[0][0] = vread(0, 0, 0); vf[0][1] = vread(0, 0, 1); vf[1][0] = vread(0, 1, 0); vf[1][1] = vread(0, 1, 1);
  const int pw = opaque(P_OFF + (pair >> 1) * 5120 + RT0 * 1024 + lane * 16 + (pair & 1) * 8);
  const int pr = opaque(P_OFF + lane * 16);
  auto make_p = [&](int t, int i) {
    const unsigned a = S[t][i][0], b = S[t][i][1];
    const float e0 = __builtin_amdgcn_exp2f(__builtin_fmaf(__uint_as_float(a << 16), CEXP, mc[i]));
    const float e1 = __builtin_amdgcn_exp2f(__builtin_fmaf(__uint_as_float(a & 0xffff0000u), CEXP, mc[i]));
    const float e2 = __builtin_amdgcn_exp2f(__builtin_fmaf(__uint_as_float(b << 16), CEXP, mc[i]));
    const float e3 = __builtin_amdgcn_exp2f(__builtin_fmaf(__uint_as_float(b & 0xffff0000u), CEXP, mc[i]));
    ls[i] += (e0 + e1) + (e2 + e3);
    asm volatile("" : "+v"(ls[i]));
    *(LDS_AS u32x2*)(lds + pw + (t & 1) * PBUF + i * 1024) = u32x2{cvtpk(e0, e1), cvtpk(e2, e3)};
    if (t == 15) {
      float s = xg_sum(ls[i]);
      if (g == 0) *(LDS_AS float*)(lds + SUM_OFF + (pair * 80 + (RT0 + i) * 16 + r) * 4) = s;
    }
  };
#pragma unroll
  for (int i = 0; i < NT; ++i) make_p(0, i);

  f32x4 O[5][2];
#pragma unroll
  for (int i = 0; i < 5; ++i) { O[i][0] = f32x4{0.f, 0.f, 0.f, 0.f}; O[i][1] = f32x4{0.f, 0.f, 0.f, 0.f}; }

  auto pv_step = [&](auto TC) {
    constexpr int t = decltype(TC)::value;
    step_sync<16 + t, (t == 0 || t == 15) ? 0 : 4>(cx);
    bf16x8 pf[10];
#pragma unroll
    for (int n = 0; n < 10; ++n) pf[n] = *(LDS_AS bf16x8*)(lds + pr + (t & 1) * PBUF + n * 1024);
    SB();
    if (t < 15) make_p(t + 1, 0);
    SB();
#pragma unroll
    for (int n = 0; n < 10; ++n) {
      const int j = n / 5, i = n % 5;
      O[i][0] = __builtin_amdgcn_mfma_f32_16x16x32_bf16(vf[j][0], pf[n], O[i][0], 0, 0, 0);
      O[i][1] = __builtin_amdgcn_mfma_f32_16x16x32_bf16(vf[j][1], pf[n], O[i][1], 0, 0, 0);
      if (n == 1 || n == 4 || n == 7) SB();
      if (n == 1) { issue_piece<16 + t + 3, 0>(cx); SB(); if (t < 15) make_p(t + 1, 1); }
      if (n == 4) {
        issue_piece<16 + t + 3, 1>(cx); SB();
        if (t < 15) { vf[0][0] = vread(t + 1, 0, 0); vf[0][1] = vread(t + 1, 0, 1); }
        if (t < 15 && NT == 3) make_p(t + 1, NT - 1);
      }
      if (n == 7) { issue_piece<16 + t + 3, 2>(cx); SB(); }
    }
    SB();
    if (t < 15) { vf[1][0] = vread(t + 1, 1, 0); vf[1][1] = vread(t + 1, 1, 1); }
    issue_piece<16 + t + 3, 3>(cx);
  };
  unroll_seq(pv_step, std::make_integer_sequence<int, 16>{});

  __hip_bfloat16* op = outp + r * 256 + wave * 32 + (g & 1) * 16 + (g >> 1) * 8;
#pragma unroll
  for (int i = 0; i < 5; ++i) {
    const int o = SUM_OFF + (i * 16 + r) * 4;
    const float s = (*(LDS_AS float*)(lds + o) + *(LDS_AS float*)(lds + o + 320)) +
                    (*(LDS_AS float*)(lds + o + 640) + *(LDS_AS float*)(lds + o + 960));
    const float inv = __builtin_amdgcn_rcpf(s);
    const unsigned a0 = cvtpk(O[i][0][0] * inv, O[i][0][1] * inv), a1 = cvtpk(O[i][0][2] * inv, O[i][0][3] * inv);
    const unsigned b0 = cvtpk(O[i][1][0] * inv, O[i][1][1] * inv), b1 = cvtpk(O[i][1][2] * inv, O[i][1][3] * inv);
    auto s0 = __builtin_amdgcn_permlane16_swap(a0, b0, false, false);
    auto s1 = __builtin_amdgcn_permlane16_swap(a1, b1, false, false);
    const u32x4 val = u32x4{s0[0], s1[0], s0[1], s1[1]};
    if (i < 4 || r < 8) {
      if (ONT) __builtin_nontemporal_store(val, (u32x4*)(op + i * 16 * 256));
      else *(u32x4*)(op + i * 16 * 256) = val;
    }
  }
}

__global__ __launch_bounds__(512, 2) void gqa_attn_kernel(const __hip_bfloat16* __restrict__ q, const __hip_bfloat16* __restrict__ k,
                                                          const __hip_bfloat16* __restrict__ v, __hip_bfloat16* __restrict__ out) {
  __shared__ __attribute__((aligned(16))) char smem[LDS_BYTES];
  LDS_AS char* lds = (LDS_AS char*)smem;
  const int bid = blockIdx.x;
  const int idx = bid >> 3;
  const int grp = (bid & 7) * 8 + (idx >> 2);
  const int c = idx & 3;
  const int wave = __builtin_amdgcn_readfirstlane(threadIdx.x >> 6);
  const int lane = threadIdx.x & 63;

  Ctx cx;
  cx.srdK = __builtin_amdgcn_make_buffer_rsrc((void*)(k + (size_t)grp * 1024 * 256), (short)0, 1024 * 512, 0x00020000);
  cx.srdV = __builtin_amdgcn_make_buffer_rsrc((void*)(v + (size_t)grp * 1024 * 256), (short)0, 1024 * 512, 0x00020000);
  const size_t qrow0 = (size_t)grp * 288 + c * 72;
  __amdgpu_buffer_rsrc_t srdQ = __builtin_amdgcn_make_buffer_rsrc((void*)(q + qrow0 * 256), (short)0, 72 * 512, 0x00020000);
  const unsigned ldsbase = (unsigned)(unsigned long long)lds;
  const unsigned lrow = (unsigned)(lane >> 3);
  const unsigned lsw = (unsigned)(((lane & 7) ^ (lrow & 6)) * 16) + lrow * 512;
  cx.voff = lsw + (unsigned)wave * 4096u;
  cx.ldsw = ldsbase + (unsigned)wave * 4096u;
  cx.c = c;

  STAMP(0);
  asm volatile("s_nop 4" ::: "memory");
  dma_rowblock(srdQ, cx.voff, 0u, cx.ldsw + Q_OFF);
  if (wave < 4) dma<0>(srdQ, lsw, 8u * 4096u + (unsigned)wave * 128u, ldsbase + Q_OFF + 8 * 4096 + (unsigned)wave * 1024u);
  issue_tile<0>(cx);
  issue_tile<1>(cx);
  issue_tile<2>(cx);

  __hip_bfloat16* outp = out + qrow0 * 256;
  if (wave < 4) body<3, 0>(lds, cx, wave, lane, outp);
  else body<2, 3>(lds, cx, wave, lane, outp);
}

}

extern "C" void kernel_launch(void* const* d_in, const int* in_sizes, int n_in, void* d_out, int out_size, void* d_ws,
                              size_t ws_size, hipStream_t stream) {
  const __hip_bfloat16* q = (const __hip_bfloat16*)d_in[0];
  const __hip_bfloat16* k = (const __hip_bfloat16*)d_in[1];
  const __hip_bfloat16* v = (const __hip_bfloat16*)d_in[2];
  gqa_attn_kernel<<<256, 512, 0, stream>>>(q, k, v, (__hip_bfloat16*)d_out);
}
```

```cpp
#include <hip/hip_runtime.h>
#include <hip/hip_bf16.h>
#include <utility>

typedef __bf16 bf16x8 __attribute__((ext_vector_type(8)));
typedef float f32x4 __attribute__((ext_vector_type(4)));
typedef short s16x4 __attribute__((ext_vector_type(4)));
typedef unsigned u32x2 __attribute__((ext_vector_type(2)));
typedef unsigned u32x4 __attribute__((ext_vector_type(4)));
#define LDS_AS __attribute__((address_space(3)))

#define ROT 0
#define LDPOL " nt"
#define ONT 1
namespace {

constexpr int SLOT = 32768;
constexpr int PBUF = 10240;
constexpr int P_OFF = 0;
constexpr int REF_OFF = 2 * PBUF;
constexpr int MAX_OFF = REF_OFF + 1280;
constexpr int SUM_OFF = MAX_OFF + 1280;
constexpr int RING = 24576;
constexpr int Q_OFF = RING + 3 * SLOT;
constexpr int LDS_BYTES = Q_OFF + 36864;
#define SB() __builtin_amdgcn_sched_barrier(0)

__device__ __forceinline__ unsigned cvtpk(float lo, float hi) {
  typedef float f32x2 __attribute__((ext_vector_type(2)));
  typedef __bf16 bf16x2 __attribute__((ext_vector_type(2)));
  return __builtin_bit_cast(unsigned, __builtin_convertvector(f32x2{lo, hi}, bf16x2));
}
template <int N> __device__ __forceinline__ void waitvm() { asm volatile("s_waitcnt vmcnt(%0)" ::"i"(N) : "memory"); }
__device__ __forceinline__ void wg_barrier() {
  asm volatile("s_waitcnt lgkmcnt(0)\n\ts_barrier" ::: "memory");
}
template <int OFF>
__device__ __forceinline__ void dma(__amdgpu_buffer_rsrc_t srd, unsigned voff, unsigned soff, unsigned m0v) {
  asm volatile("s_mov_b32 m0, %0\n\ts_nop 0\n\tbuffer_load_dwordx4 %1, %2, %3 offen offset:%4" LDPOL " lds"
               ::"s"(m0v), "v"(voff), "s"(srd), "s"(soff), "i"(OFF) : "memory");
}
__device__ __forceinline__ void dma_rowblock(__amdgpu_buffer_rsrc_t srd, unsigned voff, unsigned soff, unsigned ldsdst) {
  dma<0>(srd, voff, soff, ldsdst);
  dma<128>(srd, voff, soff, ldsdst + 896);
  dma<256>(srd, voff, soff, ldsdst + 2 * 896);
  dma<384>(srd, voff, soff, ldsdst + 3 * 896);
}
__device__ __forceinline__ float xg_max(float x) {
  auto s = __builtin_amdgcn_permlane16_swap(__float_as_uint(x), __float_as_uint(x), false, false);
  x = fmaxf(__uint_as_float(s[0]), __uint_as_float(s[1]));
  auto t = __builtin_amdgcn_permlane32_swap(__float_as_uint(x), __float_as_uint(x), false, false);
  return fmaxf(__uint_as_float(t[0]), __uint_as_float(t[1]));
}
__device__ __forceinline__ float xg_sum(float x) {
  auto s = __builtin_amdgcn_permlane16_swap(__float_as_uint(x), __float_as_uint(x), false, false);
  x = __uint_as_float(s[0]) + __uint_as_float(s[1]);
  auto t = __builtin_amdgcn_permlane32_swap(__float_as_uint(x), __float_as_uint(x), false, false);
  return __uint_as_float(t[0]) + __uint_as_float(t[1]);
}

#define STAMP(i)
struct Ctx {
  __amdgpu_buffer_rsrc_t srdQ, srdV;
  void* kp;
  unsigned voff;
  unsigned ldsw;
};

template <int SL, int PIECE> __device__ __forceinline__ void issue_piece(const Ctx& cx, bool isV, int tile) {
  const __amdgpu_buffer_rsrc_t srdK = __builtin_amdgcn_make_buffer_rsrc(cx.kp, (short)0, tile < 16 ? 1024 * 512 : 0, 0x00020000);
  dma<PIECE * 128>(isV ? cx.srdV : srdK, cx.voff, (unsigned)(tile * SLOT), (unsigned)(RING + SL * SLOT + PIECE * 896) + cx.ldsw);
}
template <int SL> __device__ __forceinline__ void issue_tile(const Ctx& cx, bool isV, int tile) {
  issue_piece<SL, 0>(cx, isV, tile); issue_piece<SL, 1>(cx, isV, tile); issue_piece<SL, 2>(cx, isV, tile); issue_piece<SL, 3>(cx, isV, tile);
}
__device__ __forceinline__ void prologue(const Ctx& cx, int wave) {
  dma_rowblock(cx.srdQ, cx.voff, 0u, cx.ldsw + Q_OFF);
  if (wave < 4) dma<0>(cx.srdQ, cx.voff, 32768u - (unsigned)wave * 3968u, cx.ldsw + Q_OFF + 32768u - (unsigned)wave * 3072u);
  issue_tile<0>(cx, false, 0);
  issue_tile<1>(cx, false, 1);
  issue_tile<2>(cx, true, 0);
}
__device__ __forceinline__ int lane_id() {
  int l;
  asm volatile("v_mbcnt_lo_u32_b32 %0, -1, 0\n\tv_mbcnt_hi_u32_b32 %0, -1, %0" : "=v"(l));
  return l;
}
template <int VM, int LG> __device__ __forceinline__ void sync() {
  SB();
  asm volatile("s_waitcnt vmcnt(%0) lgkmcnt(%1)\n\ts_barrier" ::"i"(VM), "i"(LG) : "memory");
  SB();
}
__device__ __forceinline__ int opaque(int x) { asm volatile("" : "+v"(x)); return x; }
#define IC(x) std::integral_constant<int, (x)>{}

template <int NT, int RT0>
__device__ __forceinline__ void body(LDS_AS char* lds, const Ctx& cx, int wave, int lane, __hip_bfloat16* __restrict__ outp) {
  const int pair = wave & 3;
  const int r = lane & 15, g = lane >> 4;
  constexpr float CEXP = 0.0625f * 1.4426950408889634f;
  constexpr float THR = 32.0f;
  constexpr int PD = 3;
  constexpr int PFD = 5;

  const int kbase = RING + (2 * pair + (r >> 3)) * 4096 + (r & 7) * 128;
  int kb[2][2];
  kb[0][0] = opaque(kbase + ((g ^ (r & 6)) * 16));
  kb[1][0] = opaque(kbase + (((4 + g) ^ (r & 6)) * 16));
  kb[0][1] = opaque(kb[0][0] + 2 * SLOT);
  kb[1][1] = opaque(kb[1][0] + 2 * SLOT);
  auto kread = [&](int sl, int ks) { return *(LDS_AS bf16x8*)(lds + kb[ks & 1][sl >> 1] + (sl & 1) * SLOT + (ks >> 1) * 1024); };
  const int q4 = (lane >> 2) & 3, p4 = lane & 3;
  const int r7 = 4 * (g & 1) + q4;
  const int vrow = RING + (g >> 1) * 4096 + (wave >> 1) * 1024 + r7 * 128 + (p4 & 1) * 8;
  int vb[2][2];
  vb[0][0] = opaque(vrow + (((4 * (wave & 1) + (p4 >> 1)) ^ (r7 & 6)) * 16));
  vb[1][0] = opaque(vrow + (((4 * (wave & 1) + 2 + (p4 >> 1)) ^ (r7 & 6)) * 16));
  vb[0][1] = opaque(vb[0][0] + 2 * SLOT);
  vb[1][1] = opaque(vb[1][0] + 2 * SLOT);
  auto vread = [&](int sl, int j, int dt) {
    const int o = vb[dt][sl >> 1] + j * 16384;
    const s16x4 a0 = __builtin_amdgcn_ds_read_tr16_b64_v4i16((LDS_AS s16x4*)(lds + o));
    const s16x4 a1 = __builtin_amdgcn_ds_read_tr16_b64_v4i16((LDS_AS s16x4*)(lds + o + 8192));
    return __builtin_bit_cast(bf16x8, __builtin_shufflevector(a0, a1, 0, 1, 2, 3, 4, 5, 6, 7));
  };
  const int pw = opaque(P_OFF + (pair >> 1) * 5120 + RT0 * 1024 + lane * 16 + (pair & 1) * 8);
  const int pr = opaque(P_OFF + lane * 16);

  float mx[NT], mc[NT], ls[NT];
#pragma unroll
  for (int i = 0; i < NT; ++i) mx[i] = -INFINITY;
  f32x4 O[5][2];
  float inv[5];

  for (int pass = 0;; ++pass) {
  sync<8, 0>();
  STAMP(1);
  bf16x8 qf[NT][8];
  {
  const int l2 = lane_id(), r = l2 & 15, g = l2 >> 4;
#pragma unroll
  for (int ks = 0; ks < 8; ++ks) {
#pragma unroll
    for (int i = 0; i < NT; ++i) {
      const int rt = RT0 + i;
      const int rr = (rt == 4) ? (r & 7) : r;
      const int base = Q_OFF + (2 * rt + (rr >> 3)) * 4096 + (rr & 7) * 128;
      qf[i][ks] = *(LDS_AS bf16x8*)(lds + base + (ks >> 1) * 1024 + ((((ks & 1) * 4 + g) ^ (rr & 6)) * 16));
    }
  }
  }
    bf16x8 kf[8], vf[2][2];
    f32x4 sc[NT];
    f32x4 held;
#pragma unroll
    for (int i = 0; i < 5; ++i) { O[i][0] = f32x4{0.f, 0.f, 0.f, 0.f}; O[i][1] = f32x4{0.f, 0.f, 0.f, 0.f}; }
#pragma unroll
    for (int i = 0; i < NT; ++i) ls[i] = 0.f;
#pragma unroll
    for (int ks = 0; ks < PD; ++ks) kf[ks] = kread(0, ks);

    auto make_p = [&](const f32x4 s, int i, int buf) {
      mx[i] = fmaxf(fmaxf(mx[i], s[0]), s[1]);
      mx[i] = fmaxf(fmaxf(mx[i], s[2]), s[3]);
      const float e0 = __builtin_amdgcn_exp2f(__builtin_fmaf(s[0], CEXP, mc[i]));
      const float e1 = __builtin_amdgcn_exp2f(__builtin_fmaf(s[1], CEXP, mc[i]));
      const float e2 = __builtin_amdgcn_exp2f(__builtin_fmaf(s[2], CEXP, mc[i]));
      const float e3 = __builtin_amdgcn_exp2f(__builtin_fmaf(s[3], CEXP, mc[i]));
      ls[i] += (e0 + e1) + (e2 + e3);
      asm volatile("" : "+v"(ls[i]), "+v"(mx[i]));
      *(LDS_AS u32x2*)(lds + pw + buf * PBUF + i * 1024) = u32x2{cvtpk(e0, e1), cvtpk(e2, e3)};
    };
    auto qk_step = [&](auto SLC, auto MODEC, int nxt, int pbuf) {
      constexpr int SL = decltype(SLC)::value, MODE = decltype(MODEC)::value;
      constexpr int NS = (SL + 3) & 3;
      f32x4 old[NT];
      if constexpr (MODE == 1) {
        sync<4, 0>();
        const int r = lane_id() & 15;
#pragma unroll
        for (int i = 0; i < NT; ++i) {
          const int o = REF_OFF + ((RT0 + i) * 16 + r) * 4;
          const float m = fmaxf(fmaxf(*(LDS_AS float*)(lds + o), *(LDS_AS float*)(lds + o + 320)),
                                fmaxf(*(LDS_AS float*)(lds + o + 640), *(LDS_AS float*)(lds + o + 960)));
          mc[i] = -m * CEXP;
          old[i] = sc[i];
        }
      }
      if constexpr (MODE == 2) sync<4, 15>();
#pragma unroll
      for (int ks = 0; ks < 8; ++ks) {
        if constexpr (MODE == 0) { if (ks == 8 - PD) { sync<4, 0>(); issue_tile<3>(cx, false, 2); SB(); } }
        if (ks + PD < 8) kf[ks + PD] = kread(SL, ks + PD);
        else if (MODE == 0) kf[ks + PD - 8] = kread(1, ks + PD - 8);
        if (MODE != 0 && ks == 6) { vf[0][0] = vread((SL + 1) & 3, 0, 0); vf[0][1] = vread((SL + 1) & 3, 0, 1); }
        if (MODE != 0 && ks == 7) { vf[1][0] = vread((SL + 1) & 3, 1, 0); vf[1][1] = vread((SL + 1) & 3, 1, 1); }
#pragma unroll
        for (int i = 0; i < NT; ++i)
          sc[i] = __builtin_amdgcn_mfma_f32_16x16x32_bf16(kf[ks], qf[i][ks], ks == 0 ? f32x4{0.f, 0.f, 0.f, 0.f} : sc[i], 0, 0, 0);
        if (MODE == 1 && ks >= 1 && ks <= NT) make_p(old[ks - 1], ks - 1, 0);
        if (MODE == 2 && ks == 2) make_p(held, NT - 1, pbuf);
        if (!(MODE == 2 && ks == 1)) SB();
        if constexpr (MODE != 0) {
          if (ks == 0) issue_piece<NS, 0>(cx, true, nxt);
          if (ks == 3) issue_piece<NS, 1>(cx, true, nxt);
          if (ks == 5) issue_piece<NS, 2>(cx, true, nxt);
          if (ks == 7) issue_piece<NS, 3>(cx, true, nxt);
          if (!(MODE == 2 && ks == 1)) SB();
        }
      }
      if constexpr (MODE == 0) {
        const int xo = (pair * 80 + RT0 * 16 + (lane_id() & 15)) * 4;
#pragma unroll
        for (int i = 0; i < NT; ++i) {
          float m = fmaxf(fmaxf(fmaxf(mx[i], sc[i][0]), sc[i][1]), fmaxf(sc[i][2], sc[i][3]));
          *(LDS_AS float*)(lds + REF_OFF + xo + i * 64) = xg_max(m);
        }
      }
    };
    auto pv_step = [&](auto SLC, auto PBC, auto MODEC, int nxt) {
      constexpr int SL = decltype(SLC)::value, PB = decltype(PBC)::value, MODE = decltype(MODEC)::value;
      constexpr int NS = (SL + 3) & 3;
      if constexpr (MODE == 0) sync<4, 8>();
      if constexpr (MODE == 1) sync<8, 8>();
      if constexpr (MODE == 2) {
        sync<0, 0>();
        vf[0][0] = vread(SL, 0, 0); vf[0][1] = vread(SL, 0, 1);
      }
      bf16x8 pf[10];
      auto pread = [&](int n) { return *(LDS_AS bf16x8*)(lds + pr + PB * PBUF + n * 1024); };
#pragma unroll
      for (int n = 0; n < PFD; ++n) pf[n] = pread(n);
      if (MODE == 2) { vf[1][0] = vread(SL, 1, 0); vf[1][1] = vread(SL, 1, 1); }
      SB();
      if constexpr (MODE != 2) make_p(sc[0], 0, PB ^ 1);
      if constexpr (MODE == 2) {
        bool bad = false;
        const int r = lane_id() & 15;
#pragma unroll
        for (int i = 0; i < 5; ++i) {
          const int o = (i * 16 + r) * 4;
          auto rd = [&](int b, int p) { return *(LDS_AS float*)(lds + b + o + p * 320); };
          inv[i] = __builtin_amdgcn_rcpf((rd(SUM_OFF, 0) + rd(SUM_OFF, 1)) + (rd(SUM_OFF, 2) + rd(SUM_OFF, 3)));
          const float mt = fmaxf(fmaxf(rd(MAX_OFF, 0), rd(MAX_OFF, 1)), fmaxf(rd(MAX_OFF, 2), rd(MAX_OFF, 3)));
          const float mr = fmaxf(fmaxf(rd(REF_OFF, 0), rd(REF_OFF, 1)), fmaxf(rd(REF_OFF, 2), rd(REF_OFF, 3)));
          bad |= !((mt - mr) * CEXP <= THR);
        }
        nxt = __builtin_amdgcn_ballot_w64(bad) != 0ull;
      }
      SB();
#pragma unroll
      for (int n = 0; n < 10; ++n) {
        const int j = n / 5, i = n % 5;
        if (n + PFD < 10) pf[n + PFD] = pread(n + PFD);
        if (MODE == 0 && n >= 10 - PD) kf[n - (10 - PD)] = kread((SL + 1) & 3, n - (10 - PD));
        O[i][0] = __builtin_amdgcn_mfma_f32_16x16x32_bf16(vf[j][0], pf[n], O[i][0], 0, 0, 0);
        O[i][1] = __builtin_amdgcn_mfma_f32_16x16x32_bf16(vf[j][1], pf[n], O[i][1], 0, 0, 0);
        SB();
        if constexpr (MODE == 0) {
          if (n == 1) { issue_piece<NS, 0>(cx, false, nxt); SB(); if (NT == 3) make_p(sc[1], 1, PB ^ 1); }
          if (n == 4) { issue_piece<NS, 1>(cx, false, nxt); SB(); }
          if (n == 6) { issue_piece<NS, 2>(cx, false, nxt); SB(); }
          if (n == 9) { issue_piece<NS, 3>(cx, false, nxt); SB(); }
        }
        if constexpr (MODE == 1) {
          if (n == 1) make_p(sc[1], 1, PB ^ 1);
          if (n == 4 && NT == 3) make_p(sc[NT - 1], NT - 1, PB ^ 1);
          if (n == 7) {
            const int xo = (pair * 80 + RT0 * 16 + (lane_id() & 15)) * 4;
#pragma unroll
            for (int q = 0; q < NT; ++q) {
              *(LDS_AS float*)(lds + SUM_OFF + xo + q * 64) = xg_sum(ls[q]);
              *(LDS_AS float*)(lds + MAX_OFF + xo + q * 64) = xg_max(mx[q]);
            }
          }
        }
      }
      held = sc[NT - 1];
      return nxt;
    };

    qk_step(IC(0), IC(0), 0, 0);
    qk_step(IC(1), IC(1), 1, 0);
    STAMP(2);
#pragma unroll 1
    for (int it = 0; it < 7; ++it) {
      pv_step(IC(2), IC(0), IC(0), 2 * it + 3);
      qk_step(IC(3), IC(2), 2 * it + 2, 1);
      pv_step(IC(0), IC(1), IC(0), 2 * it + 4);
      qk_step(IC(1), IC(2), 2 * it + 3, 0);
    }
    STAMP(3);
    pv_step(IC(2), IC(0), IC(1), 0);
    const int redo = pv_step(IC(0), IC(1), IC(2), 0);
    STAMP(4);
    if (!redo || pass == 1) break;
    sync<0, 0>();
    prologue(cx, wave);
  }

  const int l3 = lane_id(), r3 = l3 & 15, g3 = l3 >> 4;
  __hip_bfloat16* op = outp + r3 * 256 + wave * 32 + (g3 & 1) * 16 + (g3 >> 1) * 8;
#pragma unroll
  for (int i = 0; i < 5; ++i) {
    const unsigned a0 = cvtpk(O[i][0][0] * inv[i], O[i][0][1] * inv[i]), a1 = cvtpk(O[i][0][2] * inv[i], O[i][0][3] * inv[i]);
    const unsigned b0 = cvtpk(O[i][1][0] * inv[i], O[i][1][1] * inv[i]), b1 = cvtpk(O[i][1][2] * inv[i], O[i][1][3] * inv[i]);
    auto s0 = __builtin_amdgcn_permlane16_swap(a0, b0, false, false);
    auto s1 = __builtin_amdgcn_permlane16_swap(a1, b1, false, false);
    const u32x4 val = u32x4{s0[0], s1[0], s0[1], s1[1]};
    if (i < 4 || r3 < 8) {
      if (ONT) __builtin_nontemporal_store(val, (u32x4*)(op + i * 16 * 256));
      else *(u32x4*)(op + i * 16 * 256) = val;
    }
  }
}

__global__ __launch_bounds__(512, 2) void gqa_attn_kernel(const __hip_bfloat16* __restrict__ q, const __hip_bfloat16* __restrict__ k,
                                                          const __hip_bfloat16* __restrict__ v, __hip_bfloat16* __restrict__ out) {
  __shared__ __attribute__((aligned(16))) char smem[LDS_BYTES];
  LDS_AS char* lds = (LDS_AS char*)smem;
  const int bid = blockIdx.x;
  const int idx = bid >> 3;
  const int grp = (bid & 7) * 8 + (idx >> 2);
  const int c = idx & 3;
  const int wave = __builtin_amdgcn_readfirstlane(threadIdx.x >> 6);
  const int lane = threadIdx.x & 63;

  Ctx cx;
  cx.kp = (void*)(k + (size_t)grp * 1024 * 256);
  cx.srdV = __builtin_amdgcn_make_buffer_rsrc((void*)(v + (size_t)grp * 1024 * 256), (short)0, 1024 * 512, 0x00020000);
  const size_t qrow0 = (size_t)grp * 288 + c * 72;
  cx.srdQ = __builtin_amdgcn_make_buffer_rsrc((void*)(q + qrow0 * 256), (short)0, 72 * 512, 0x00020000);
  const unsigned ldsbase = (unsigned)(unsigned long long)lds;
  const unsigned lrow = (unsigned)(lane >> 3);
  const unsigned lsw = (unsigned)(((lane & 7) ^ (lrow & 6)) * 16) + lrow * 512;
  cx.voff = lsw + (unsigned)wave * 4096u;
  cx.ldsw = ldsbase + (unsigned)wave * 4096u;

  STAMP(0);
  asm volatile("s_nop 4" ::: "memory");
  prologue(cx, wave);

  __hip_bfloat16* outp = out + qrow0 * 256;
  if (wave < 4) body<3, 0>(lds, cx, wave, lane, outp);
  else body<2, 3>(lds, cx, wave, lane, outp);
}

}

extern "C" void kernel_launch(void* const* d_in, const int* in_sizes, int n_in, void* d_out, int out_size, void* d_ws,
                              size_t ws_size, hipStream_t stream) {
  const __hip_bfloat16* q = (const __hip_bfloat16*)d_in[0];
  const __hip_bfloat16* k = (const __hip_bfloat16*)d_in[1];
  const __hip_bfloat16* v = (const __hip_bfloat16*)d_in[2];
  gqa_attn_kernel<<<256, 512, 0, stream>>>(q, k, v, (__hip_bfloat16*)d_out);
}
```

```cpp
#include <hip/hip_runtime.h>
#include <hip/hip_bf16.h>
#include <utility>

typedef __bf16 bf16x8 __attribute__((ext_vector_type(8)));
typedef float f32x4 __attribute__((ext_vector_type(4)));
typedef short s16x4 __attribute__((ext_vector_type(4)));
typedef unsigned u32x2 __attribute__((ext_vector_type(2)));
typedef unsigned u32x4 __attribute__((ext_vector_type(4)));
#define LDS_AS __attribute__((address_space(3)))

#define ROT 0
#define LDPOL " nt"
#define DA 2
#define ONT 1
namespace {

constexpr int SLOT = 32768;
constexpr int PBUF = 10240;
constexpr int P_OFF = 0;
constexpr int REF_OFF = 2 * PBUF;
constexpr int SUM_OFF = REF_OFF + 1280;
constexpr int FLG_OFF = SUM_OFF + 1280;
constexpr int RING = 24576;
constexpr int Q_OFF = RING + 3 * SLOT;
constexpr int LDS_BYTES = Q_OFF + 36864;
#define SB() __builtin_amdgcn_sched_barrier(0)
#define IC(x) std::integral_constant<int, (x)>{}
template <int N, class F> __device__ __forceinline__ void static_for(F&& f) {
  [&]<int... I>(std::integer_sequence<int, I...>) { (f(IC(I)), ...); }(std::make_integer_sequence<int, N>{});
}

__device__ __forceinline__ unsigned cvtpk(float lo, float hi) {
  typedef float f32x2 __attribute__((ext_vector_type(2)));
  typedef __bf16 bf16x2 __attribute__((ext_vector_type(2)));
  return __builtin_bit_cast(unsigned, __builtin_convertvector(f32x2{lo, hi}, bf16x2));
}
template <int N> __device__ __forceinline__ void waitvm() { asm volatile("s_waitcnt vmcnt(%0)" ::"i"(N) : "memory"); }
__device__ __forceinline__ void wg_barrier() {
  asm volatile("s_waitcnt lgkmcnt(0)\n\ts_barrier" ::: "memory");
}
template <int OFF>
__device__ __forceinline__ void dma(__amdgpu_buffer_rsrc_t srd, unsigned voff, unsigned soff, unsigned m0v) {
  asm volatile("s_mov_b32 m0, %0\n\ts_nop 0\n\tbuffer_load_dwordx4 %1, %2, %3 offen offset:%4" LDPOL " lds"
               ::"s"(m0v), "v"(voff), "s"(srd), "s"(soff), "i"(OFF) : "memory");
}
__device__ __forceinline__ void dma_rowblock(__amdgpu_buffer_rsrc_t srd, unsigned voff, unsigned soff, unsigned ldsdst) {
  dma<0>(srd, voff, soff, ldsdst);
  dma<128>(srd, voff, soff, ldsdst + 896);
  dma<256>(srd, voff, soff, ldsdst + 2 * 896);
  dma<384>(srd, voff, soff, ldsdst + 3 * 896);
}
__device__ __forceinline__ float xg_max(float x) {
  auto s = __builtin_amdgcn_permlane16_swap(__float_as_uint(x), __float_as_uint(x), false, false);
  x = fmaxf(__uint_as_float(s[0]), __uint_as_float(s[1]));
  auto t = __builtin_amdgcn_permlane32_swap(__float_as_uint(x), __float_as_uint(x), false, false);
  return fmaxf(__uint_as_float(t[0]), __uint_as_float(t[1]));
}
__device__ __forceinline__ float xg_sum(float x) {
  auto s = __builtin_amdgcn_permlane16_swap(__float_as_uint(x), __float_as_uint(x), false, false);
  x = __uint_as_float(s[0]) + __uint_as_float(s[1]);
  auto t = __builtin_amdgcn_permlane32_swap(__float_as_uint(x), __float_as_uint(x), false, false);
  return __uint_as_float(t[0]) + __uint_as_float(t[1]);
}

#define STAMP(i)
struct Ctx {
  __amdgpu_buffer_rsrc_t srdQ, srdV;
  void* kp;
  unsigned voff;
  unsigned ldsw;
};

template <int SL, int PIECE> __device__ __forceinline__ void issue_piece(const Ctx& cx, bool isV, int tile) {
  const __amdgpu_buffer_rsrc_t srdK = __builtin_amdgcn_make_buffer_rsrc(cx.kp, (short)0, tile < 16 ? 1024 * 512 : 0, 0x00020000);
  dma<PIECE * 128>(isV ? cx.srdV : srdK, cx.voff, (unsigned)(tile * SLOT), (unsigned)(RING + SL * SLOT + PIECE * 896) + cx.ldsw);
}
template <int NT, int SL, int G> __device__ __forceinline__ void issue_at(const Ctx& cx, bool isV, int tile) {
  constexpr int posA[5][4] = {{9, 9, 9, 9}, {3, 9, 9, 9}, {3, 7, 9, 9}, {1, 3, 6, 9}, {1, 3, 5, 7}};
  constexpr int posB[5][8] = {{0, 1, 2, 3, 4, 5, 6, 7}, {0, 1, 2, 4, 5, 6, 7, 9}, {0, 1, 2, 4, 5, 6, 9, 9}, {0, 2, 4, 5, 7, 9, 9, 9}, {0, 2, 4, 6, 9, 9, 9, 9}};
  const __amdgpu_buffer_rsrc_t srdK = __builtin_amdgcn_make_buffer_rsrc(cx.kp, (short)0, tile < 16 ? 1024 * 512 : 0, 0x00020000);
  const unsigned so = (unsigned)(tile * SLOT), ld = (unsigned)(RING + SL * SLOT) + cx.ldsw;
  auto one = [&](auto PC) {
    constexpr int P = decltype(PC)::value;
    if constexpr (NT == 3) { if constexpr (P < 4) if constexpr (posA[DA][P] == G) { dma<P * 128>(isV ? cx.srdV : srdK, cx.voff, so, ld + P * 896); SB(); } }
    else if constexpr (posB[DA][P] == G) {
      if constexpr (P < 4) dma<P * 128>(isV ? cx.srdV : srdK, cx.voff, so, ld + P * 896);
      else dma<(P - 4 + DA) * 128>(isV ? cx.srdV : srdK, cx.voff, so - 16384u, ld - 16384u + (P - 4 + DA) * 896);
      SB();
    }
  };
  one(IC(0)); one(IC(1)); one(IC(2)); one(IC(3)); one(IC(4)); one(IC(5)); one(IC(6)); one(IC(7));
}
template <int SL> __device__ __forceinline__ void issue_tile(const Ctx& cx, bool isV, int tile) {
  issue_piece<SL, 0>(cx, isV, tile); issue_piece<SL, 1>(cx, isV, tile); issue_piece<SL, 2>(cx, isV, tile); issue_piece<SL, 3>(cx, isV, tile);
}
__device__ __forceinline__ void prologue(const Ctx& cx, int wave) {
  dma_rowblock(cx.srdQ, cx.voff, 0u, cx.ldsw + Q_OFF);
  if (wave < 4) dma<0>(cx.srdQ, cx.voff, 32768u - (unsigned)wave * 3968u, cx.ldsw + Q_OFF + 32768u - (unsigned)wave * 3072u);
  issue_tile<0>(cx, false, 0);
  issue_tile<1>(cx, false, 1);
  issue_tile<2>(cx, true, 0);
}
__device__ __forceinline__ int lane_id() {
  int l;
  asm volatile("v_mbcnt_lo_u32_b32 %0, -1, 0\n\tv_mbcnt_hi_u32_b32 %0, -1, %0" : "=v"(l));
  return l;
}
template <int VM, int LG> __device__ __forceinline__ void sync() {
  SB();
  asm volatile("s_waitcnt vmcnt(%0) lgkmcnt(%1)\n\ts_barrier" ::"i"(VM), "i"(LG) : "memory");
  SB();
}
__device__ __forceinline__ int opaque(int x) { asm volatile("" : "+v"(x)); return x; }

template <int NT, int RT0>
__device__ __forceinline__ void body(LDS_AS char* lds, const Ctx& cx, int wave, int lane, __hip_bfloat16* __restrict__ outp) {
  const int pair = wave & 3;
  const int r = lane & 15, g = lane >> 4;
  constexpr float CEXP = 0.0625f * 1.4426950408889634f;
  constexpr float THR = 32.0f;
  constexpr int PD = 3;
  constexpr int VS = NT == 3 ? DA : 8 - DA;

  const int kbase = RING + (2 * pair + (r >> 3)) * 4096 + (r & 7) * 128;
  int kb[2][2];
  kb[0][0] = opaque(kbase + ((g ^ (r & 6)) * 16));
  kb[1][0] = opaque(kbase + (((4 + g) ^ (r & 6)) * 16));
  kb[0][1] = opaque(kb[0][0] + 2 * SLOT);
  kb[1][1] = opaque(kb[1][0] + 2 * SLOT);
  auto kread = [&](int sl, int ks) { return *(LDS_AS bf16x8*)(lds + kb[ks & 1][sl >> 1] + (sl & 1) * SLOT + (ks >> 1) * 1024); };
  const int q4 = (lane >> 2) & 3, p4 = lane & 3;
  const int r7 = 4 * (g & 1) + q4;
  const int vrow = RING + (g >> 1) * 4096 + (wave >> 1) * 1024 + r7 * 128 + (p4 & 1) * 8;
  int vb[2][2];
  vb[0][0] = opaque(vrow + (((4 * (wave & 1) + (p4 >> 1)) ^ (r7 & 6)) * 16));
  vb[1][0] = opaque(vrow + (((4 * (wave & 1) + 2 + (p4 >> 1)) ^ (r7 & 6)) * 16));
  vb[0][1] = opaque(vb[0][0] + 2 * SLOT);
  vb[1][1] = opaque(vb[1][0] + 2 * SLOT);
  auto vread = [&](int sl, int j, int dt) {
    const int o = vb[dt][sl >> 1] + j * 16384;
    const s16x4 a0 = __builtin_amdgcn_ds_read_tr16_b64_v4i16((LDS_AS s16x4*)(lds + o));
    const s16x4 a1 = __builtin_amdgcn_ds_read_tr16_b64_v4i16((LDS_AS s16x4*)(lds + o + 8192));
    return __builtin_bit_cast(bf16x8, __builtin_shufflevector(a0, a1, 0, 1, 2, 3, 4, 5, 6, 7));
  };
  const int pw = opaque(P_OFF + (pair >> 1) * 5120 + RT0 * 1024 + lane * 16 + (pair & 1) * 8);
  const int pr = opaque(P_OFF + lane * 16);

  float mx[NT], mc[NT], ls[NT];
#pragma unroll
  for (int i = 0; i < NT; ++i) mx[i] = -INFINITY;
  f32x4 O[5][2];
  float inv[5];

  for (int pass = 0;; ++pass) {
  sync<8, 0>();
  STAMP(1);
  bf16x8 qf[NT][8];
  {
  const int l2 = lane_id(), r = l2 & 15, g = l2 >> 4;
#pragma unroll
  for (int ks = 0; ks < 8; ++ks) {
#pragma unroll
    for (int i = 0; i < NT; ++i) {
      const int rt = RT0 + i;
      const int rr = (rt == 4) ? (r & 7) : r;
      const int base = Q_OFF + (2 * rt + (rr >> 3)) * 4096 + (rr & 7) * 128;
      qf[i][ks] = *(LDS_AS bf16x8*)(lds + base + (ks >> 1) * 1024 + ((((ks & 1) * 4 + g) ^ (rr & 6)) * 16));
    }
  }
  }
    bf16x8 kf[8], vf[2][2], pf[10];
    f32x4 sc[NT];
    f32x4 old[NT];
    float e[4];
#pragma unroll
    for (int i = 0; i < 5; ++i) { O[i][0] = f32x4{0.f, 0.f, 0.f, 0.f}; O[i][1] = f32x4{0.f, 0.f, 0.f, 0.f}; }
#pragma unroll
    for (int i = 0; i < NT; ++i) ls[i] = 0.f;
#pragma unroll
    for (int ks = 0; ks < PD; ++ks) kf[ks] = kread(0, ks);
    auto pread = [&](int buf, int n) { return *(LDS_AS bf16x8*)(lds + pr + buf * PBUF + n * 1024); };

    auto micro = [&](int m, const f32x4& s, int i, int buf) {
      if (m == 0) { e[0] = __builtin_amdgcn_exp2f(__builtin_fmaf(s[0], CEXP, mc[i])); e[1] = __builtin_fmaf(s[1], CEXP, mc[i]); }
      if (m == 1) { e[1] = __builtin_amdgcn_exp2f(e[1]); e[2] = __builtin_fmaf(s[2], CEXP, mc[i]); }
      if (m == 2) { e[2] = __builtin_amdgcn_exp2f(e[2]); e[3] = __builtin_fmaf(s[3], CEXP, mc[i]); }
      if (m == 3) { e[3] = __builtin_amdgcn_exp2f(e[3]); mx[i] = fmaxf(fmaxf(mx[i], s[0]), s[1]); }
      if (m == 4) { mx[i] = fmaxf(fmaxf(mx[i], s[2]), s[3]); ls[i] += (e[0] + e[1]) + (e[2] + e[3]); asm volatile("" : "+v"(ls[i]), "+v"(mx[i])); }
      if (m == 5) *(LDS_AS u32x2*)(lds + pw + buf * PBUF + i * 1024) = u32x2{cvtpk(e[0], e[1]), cvtpk(e[2], e[3])};
      SB();
    };
    auto stage_pv = [&](int sl, int buf, int part) {
      if (part == 0) pf[0] = pread(buf, 0);
      if (part == 1) { vf[0][0] = vread(sl, 0, 0); vf[0][1] = vread(sl, 0, 1); pf[1] = pread(buf, 1); }
      if (part == 2) { pf[2] = pread(buf, 2); pf[3] = pread(buf, 3); }
      if (part == 3) pf[4] = pread(buf, 4);
    };
    auto qk_step = [&](auto SLC, auto MODEC, auto PBC, int u) {
      constexpr int SL = decltype(SLC)::value, MODE = decltype(MODEC)::value, PB = decltype(PBC)::value;
      constexpr int NS = (SL + 3) & 3;
      static_for<8>([&](auto KC) {
        constexpr int ks = decltype(KC)::value;
        if constexpr (MODE == 0 && ks == 8 - PD) { sync<4, 0>(); static_for<9>([&](auto G) { issue_at<NT, 3, decltype(G)::value>(cx, false, 2); }); }
        if constexpr (MODE == 1 && ks == 2) {
          sync<VS, 2>();
          const int o = REF_OFF + (RT0 * 16 + (lane_id() & 15)) * 16;
#pragma unroll
          for (int i = 0; i < NT; ++i) {
            const f32x4 m = *(LDS_AS f32x4*)(lds + o + i * 256);
            mc[i] = -fmaxf(fmaxf(m[0], m[1]), fmaxf(m[2], m[3])) * CEXP;
          }
        }
        if constexpr (MODE == 2 && ks == 4) sync<VS, 1>();
        if constexpr (ks + PD < 8) kf[ks + PD] = kread(SL, ks + PD);
        else if constexpr (MODE == 0) kf[ks + PD - 8] = kread(1, ks + PD - 8);
        if constexpr (MODE == 2 && ks >= 4) stage_pv((SL + 1) & 3, PB, ks - 4);
        SB();
#pragma unroll
        for (int i = 0; i < NT; ++i) {
          sc[i] = __builtin_amdgcn_mfma_f32_16x16x32_bf16(kf[ks], qf[i][ks], ks == 0 ? f32x4{0.f, 0.f, 0.f, 0.f} : sc[i], 0, 0, 0);
          SB();
          const int x = ks * NT + i;
          if (MODE == 2 && x < 6) micro(x, old[NT - 1], NT - 1, PB);
          if (MODE == 1 && ks >= 2) micro((x - 2 * NT) % 6, old[(x - 2 * NT) / 6], (x - 2 * NT) / 6, PB);
        }
        if constexpr (MODE == 2 && ks < 4) issue_at<NT, SL ^ 2, 5 + ks>(cx, false, u + 1);
        if constexpr (MODE != 0 && ks >= 4) issue_at<NT, NS, ks - 4>(cx, true, u);
      });
      if constexpr (MODE == 0) {
        const int xo = REF_OFF + (RT0 * 16 + (lane_id() & 15)) * 16 + pair * 4;
#pragma unroll
        for (int i = 0; i < NT; ++i) {
          float m = fmaxf(fmaxf(fmaxf(mx[i], sc[i][0]), sc[i][1]), fmaxf(sc[i][2], sc[i][3]));
          *(LDS_AS float*)(lds + xo + i * 256) = xg_max(m);
          old[i] = sc[i];
        }
        SB();
      }
      if constexpr (MODE == 1) {
        sync<63, 0>();
        stage_pv(2, 0, 0); stage_pv(2, 0, 1); stage_pv(2, 0, 2); stage_pv(2, 0, 3);
        SB();
      }
    };
    auto pv_step = [&](auto SLC, auto PBC, auto MODEC, int t) {
      constexpr int SL = decltype(SLC)::value, PB = decltype(PBC)::value, MODE = decltype(MODEC)::value;
      constexpr int NS = (SL + 3) & 3;
      static_for<10>([&](auto NC) {
        constexpr int n = decltype(NC)::value;
        constexpr int j = n / 5, i = n % 5;
        if constexpr (MODE == 0 && n == 5) sync<VS, 15>();
        if constexpr (MODE == 1 && n == 8) sync<0, 0>();
        if constexpr (n < 2) vf[1][n] = vread(SL, 1, n);
        if constexpr (n < 5) pf[n + 5] = pread(PB, n + 5);
        if constexpr (MODE == 0 && n >= 10 - PD) kf[n - (10 - PD)] = kread((SL + 1) & 3, n - (10 - PD));
        if constexpr (MODE == 1 && n >= 8) { stage_pv(SL ^ 2, PB ^ 1, 2 * (n - 8)); stage_pv(SL ^ 2, PB ^ 1, 2 * (n - 8) + 1); }
        if constexpr (MODE == 2 && n == 2) {
          const int l = lane_id();
          t = __builtin_amdgcn_ballot_w64(*(LDS_AS unsigned*)(lds + FLG_OFF + (l & 7) * 4) != 0u) != 0ull;
#pragma unroll
          for (int q = 0; q < 5; ++q) {
            const f32x4 sm = *(LDS_AS f32x4*)(lds + SUM_OFF + (l & 15) * 16 + q * 256);
            inv[q] = __builtin_amdgcn_rcpf((sm[0] + sm[1]) + (sm[2] + sm[3]));
          }
        } else SB();
#pragma unroll
        for (int h = 0; h < 2; ++h) {
          O[i][h] = __builtin_amdgcn_mfma_f32_16x16x32_bf16(vf[j][h], pf[n], O[i][h], 0, 0, 0);
          if (!(MODE == 2 && n >= 2 && n < 5)) SB();
          const int x = 2 * n + h;
          if (MODE == 0 && x >= 1 && x < 7) micro(x - 1, sc[0], 0, PB ^ 1);
          if (MODE == 0 && NT == 3 && x >= 10 && x < 16) micro(x - 10, sc[1], 1, PB ^ 1);
          if (MODE == 1 && x >= 1 && x <= 3 * NT) { micro((2 * x - 2) % 6, sc[(x - 1) / 3], (x - 1) / 3, PB ^ 1); micro((2 * x - 1) % 6, sc[(x - 1) / 3], (x - 1) / 3, PB ^ 1); }
          if (MODE == 1 && x > 3 * NT && x <= 4 * NT) {
            const int q = x - 3 * NT - 1;
            *(LDS_AS float*)(lds + SUM_OFF + (RT0 * 16 + (lane_id() & 15)) * 16 + pair * 4 + q * 256) = xg_sum(ls[q]);
            SB();
          }
          if (MODE == 1 && x == 4 * NT + 1) {
            bool bad = false;
#pragma unroll
            for (int q = 0; q < NT; ++q) bad |= !(__builtin_fmaf(mx[q], CEXP, mc[q]) <= THR);
            *(LDS_AS unsigned*)(lds + FLG_OFF + wave * 4) = __builtin_amdgcn_ballot_w64(bad) != 0ull;
            SB();
          }
        }
        if constexpr (MODE != 2 && n < 5) issue_at<NT, SL ^ 2, 4 + n>(cx, true, t + 1);
        if constexpr (MODE == 0 && n >= 5) issue_at<NT, NS, n - 5>(cx, false, t + 3);
      });
      old[NT - 1] = sc[NT - 1];
      return t;
    };

    qk_step(IC(0), IC(0), IC(0), 0);
    qk_step(IC(1), IC(1), IC(0), 1);
    STAMP(2);
#pragma unroll 1
    for (int it = 0; it < 14; it += 2) {
      pv_step(IC(2), IC(0), IC(0), it);
      qk_step(IC(3), IC(2), IC(1), it + 2);
      pv_step(IC(0), IC(1), IC(0), it + 1);
      qk_step(IC(1), IC(2), IC(0), it + 3);
    }
    STAMP(3);
    pv_step(IC(2), IC(0), IC(1), 14);
    const int redo = pv_step(IC(0), IC(1), IC(2), 15);
    STAMP(4);
    if (!redo || pass == 1) break;
    sync<0, 0>();
    prologue(cx, wave);
  }

  const int l3 = lane_id(), r3 = l3 & 15, g3 = l3 >> 4;
  __hip_bfloat16* op = outp + r3 * 256 + wave * 32 + (g3 & 1) * 16 + (g3 >> 1) * 8;
#pragma unroll
  for (int i = 0; i < 5; ++i) {
    const unsigned a0 = cvtpk(O[i][0][0] * inv[i], O[i][0][1] * inv[i]), a1 = cvtpk(O[i][0][2] * inv[i], O[i][0][3] * inv[i]);
    const unsigned b0 = cvtpk(O[i][1][0] * inv[i], O[i][1][1] * inv[i]), b1 = cvtpk(O[i][1][2] * inv[i], O[i][1][3] * inv[i]);
    auto s0 = __builtin_amdgcn_permlane16_swap(a0, b0, false, false);
    auto s1 = __builtin_amdgcn_permlane16_swap(a1, b1, false, false);
    const u32x4 val = u32x4{s0[0], s1[0], s0[1], s1[1]};
    if (i < 4 || r3 < 8) {
      if (ONT) __builtin_nontemporal_store(val, (u32x4*)(op + i * 16 * 256));
      else *(u32x4*)(op + i * 16 * 256) = val;
    }
  }
}

__global__ __launch_bounds__(512, 2) void gqa_attn_kernel(const __hip_bfloat16* __restrict__ q, const __hip_bfloat16* __restrict__ k,
                                                          const __hip_bfloat16* __restrict__ v, __hip_bfloat16* __restrict__ out) {
  __shared__ __attribute__((aligned(16))) char smem[LDS_BYTES];
  LDS_AS char* lds = (LDS_AS char*)smem;
  const int bid = blockIdx.x;
  const int idx = bid >> 3;
  const int grp = (bid & 7) * 8 + (idx >> 2);
  const int c = idx & 3;
  const int wave = __builtin_amdgcn_readfirstlane(threadIdx.x >> 6);
  const int lane = threadIdx.x & 63;

  Ctx cx;
  cx.kp = (void*)(k + (size_t)grp * 1024 * 256);
  cx.srdV = __builtin_amdgcn_make_buffer_rsrc((void*)(v + (size_t)grp * 1024 * 256), (short)0, 1024 * 512, 0x00020000);
  const size_t qrow0 = (size_t)grp * 288 + c * 72;
  cx.srdQ = __builtin_amdgcn_make_buffer_rsrc((void*)(q + qrow0 * 256), (short)0, 72 * 512, 0x00020000);
  const unsigned ldsbase = (unsigned)(unsigned long long)lds;
  const unsigned lrow = (unsigned)(lane >> 3);
  const unsigned lsw = (unsigned)(((lane & 7) ^ (lrow & 6)) * 16) + lrow * 512;
  cx.voff = lsw + (unsigned)wave * 4096u;
  cx.ldsw = ldsbase + (unsigned)wave * 4096u;

  STAMP(0);
  asm volatile("s_nop 4" ::: "memory");
  prologue(cx, wave);

  __hip_bfloat16* outp = out + qrow0 * 256;
  if (wave < 4) body<3, 0>(lds, cx, wave, lane, outp);
  else body<2, 3>(lds, cx, wave, lane, outp);
}

}

extern "C" void kernel_launch(void* const* d_in, const int* in_sizes, int n_in, void* d_out, int out_size, void* d_ws,
                              size_t ws_size, hipStream_t stream) {
  const __hip_bfloat16* q = (const __hip_bfloat16*)d_in[0];
  const __hip_bfloat16* k = (const __hip_bfloat16*)d_in[1];
  const __hip_bfloat16* v = (const __hip_bfloat16*)d_in[2];
  gqa_attn_kernel<<<256, 512, 0, stream>>>(q, k, v, (__hip_bfloat16*)d_out);
}
```

```cpp
#include <hip/hip_runtime.h>
#include <hip/hip_bf16.h>
#include <utility>

typedef __bf16 bf16x8 __attribute__((ext_vector_type(8)));
typedef float f32x4 __attribute__((ext_vector_type(4)));
typedef short s16x4 __attribute__((ext_vector_type(4)));
typedef unsigned u32x2 __attribute__((ext_vector_type(2)));
typedef unsigned u32x4 __attribute__((ext_vector_type(4)));
#define LDS_AS __attribute__((address_space(3)))

#define ROT 0
#define LDPOL " nt"
#define ONT 1
namespace {

constexpr int SLOT = 32768;
constexpr int PBUF = 10240;
constexpr int P_OFF = 0;
constexpr int REF_OFF = 2 * PBUF;
constexpr int SUM_OFF = REF_OFF + 1280;
constexpr int FLG_OFF = SUM_OFF + 1280;
constexpr int RING = 24576;
constexpr int Q_OFF = RING + 3 * SLOT;
constexpr int LDS_BYTES = Q_OFF + 40960;
#define SB() __builtin_amdgcn_sched_barrier(0)
#define IC(x) std::integral_constant<int, (x)>{}
template <int N, class F> __device__ __forceinline__ void static_for(F&& f) {
  [&]<int... I>(std::integer_sequence<int, I...>) { (f(IC(I)), ...); }(std::make_integer_sequence<int, N>{});
}

__device__ __forceinline__ unsigned cvtpk(float lo, float hi) {
  typedef float f32x2 __attribute__((ext_vector_type(2)));
  typedef __bf16 bf16x2 __attribute__((ext_vector_type(2)));
  return __builtin_bit_cast(unsigned, __builtin_convertvector(f32x2{lo, hi}, bf16x2));
}
template <int N> __device__ __forceinline__ void waitvm() { asm volatile("s_waitcnt vmcnt(%0)" ::"i"(N) : "memory"); }
__device__ __forceinline__ void wg_barrier() {
  asm volatile("s_waitcnt lgkmcnt(0)\n\ts_barrier" ::: "memory");
}
template <int OFF>
__device__ __forceinline__ void dma(__amdgpu_buffer_rsrc_t srd, unsigned voff, unsigned soff, unsigned m0v) {
  asm volatile("s_mov_b32 m0, %0\n\ts_nop 0\n\tbuffer_load_dwordx4 %1, %2, %3 offen offset:%4" LDPOL " lds"
               ::"s"(m0v), "v"(voff), "s"(srd), "s"(soff), "i"(OFF) : "memory");
}
__device__ __forceinline__ void dma_rowblock(__amdgpu_buffer_rsrc_t srd, unsigned voff, unsigned soff, unsigned ldsdst) {
  dma<0>(srd, voff, soff, ldsdst);
  dma<128>(srd, voff, soff, ldsdst + 896);
  dma<256>(srd, voff, soff, ldsdst + 2 * 896);
  dma<384>(srd, voff, soff, ldsdst + 3 * 896);
}
__device__ __forceinline__ float xg_max(float x) {
  auto s = __builtin_amdgcn_permlane16_swap(__float_as_uint(x), __float_as_uint(x), false, false);
  x = fmaxf(__uint_as_float(s[0]), __uint_as_float(s[1]));
  auto t = __builtin_amdgcn_permlane32_swap(__float_as_uint(x), __float_as_uint(x), false, false);
  return fmaxf(__uint_as_float(t[0]), __uint_as_float(t[1]));
}
__device__ __forceinline__ float xg_sum(float x) {
  auto s = __builtin_amdgcn_permlane16_swap(__float_as_uint(x), __float_as_uint(x), false, false);
  x = __uint_as_float(s[0]) + __uint_as_float(s[1]);
  auto t = __builtin_amdgcn_permlane32_swap(__float_as_uint(x), __float_as_uint(x), false, false);
  return __uint_as_float(t[0]) + __uint_as_float(t[1]);
}

#define STAMP(i)
struct Ctx {
  __amdgpu_buffer_rsrc_t srdQ, srdV;
  void* kp;
  unsigned voff;
  unsigned ldsw;
};

template <int SL, int PIECE> __device__ __forceinline__ void issue_piece(const Ctx& cx, bool isV, int tile) {
  const __amdgpu_buffer_rsrc_t srdK = __builtin_amdgcn_make_buffer_rsrc(cx.kp, (short)0, tile < 16 ? 1024 * 512 : 0, 0x00020000);
  dma<PIECE * 128>(isV ? cx.srdV : srdK, cx.voff, (unsigned)(tile * SLOT), (unsigned)(RING + SL * SLOT + PIECE * 896) + cx.ldsw);
}
template <bool HI, int SL, int G> __device__ __forceinline__ void issue_at(const Ctx& cx, bool isV, int tile) {
  constexpr int pos[2][4] = {{1, 3, 5, 7}, {0, 2, 6, 7}};
  static_for<4>([&](auto PC) {
    constexpr int P = decltype(PC)::value;
    if constexpr (pos[HI][P] == G) { issue_piece<SL, P>(cx, isV, tile); SB(); }
  });
}
template <int SL> __device__ __forceinline__ void issue_tile(const Ctx& cx, bool isV, int tile) {
  issue_piece<SL, 0>(cx, isV, tile); issue_piece<SL, 1>(cx, isV, tile); issue_piece<SL, 2>(cx, isV, tile); issue_piece<SL, 3>(cx, isV, tile);
}
template <int R> __device__ __forceinline__ void prologue(const Ctx& cx, int wave) {
  dma_rowblock(cx.srdQ, cx.voff, 0u, cx.ldsw + Q_OFF);
  if (R == 5) dma<0>(cx.srdQ, cx.voff, (unsigned)(32768 + (wave >> 2) * 4096 + (wave & 3) * 128 - wave * 4096), cx.ldsw + Q_OFF + 32768u - (unsigned)wave * 3072u);
  issue_tile<0>(cx, false, 0);
  issue_tile<1>(cx, false, 1);
  issue_tile<2>(cx, true, 0);
}
__device__ __forceinline__ int lane_id() {
  int l;
  asm volatile("v_mbcnt_lo_u32_b32 %0, -1, 0\n\tv_mbcnt_hi_u32_b32 %0, -1, %0" : "=v"(l));
  return l;
}
template <int VM, int LG> __device__ __forceinline__ void sync() {
  SB();
  asm volatile("s_waitcnt vmcnt(%0) lgkmcnt(%1)\n\ts_barrier" ::"i"(VM), "i"(LG) : "memory");
  SB();
}
__device__ __forceinline__ int opaque(int x) { asm volatile("" : "+v"(x)); return x; }

template <int NT, int RT0, int R>
__device__ __forceinline__ void body(LDS_AS char* lds, const Ctx& cx, int wave, int lane, __hip_bfloat16* __restrict__ outp) {
  const int pair = wave & 3;
  const int r = lane & 15, g = lane >> 4;
  constexpr float CEXP = 0.0625f * 1.4426950408889634f;
  constexpr float THR = 32.0f;
  constexpr int PD = 3;
  constexpr int VS = 4;
  constexpr bool HI = RT0 != 0;
  STAMP(0);
  prologue<R>(cx, wave);

  const int kbase = RING + (2 * pair + (r >> 3)) * 4096 + (r & 7) * 128;
  int kb[2][2];
  kb[0][0] = opaque(kbase + ((g ^ (r & 6)) * 16));
  kb[1][0] = opaque(kbase + (((4 + g) ^ (r & 6)) * 16));
  kb[0][1] = opaque(kb[0][0] + 2 * SLOT);
  kb[1][1] = opaque(kb[1][0] + 2 * SLOT);
  auto kread = [&](int sl, int ks) { return *(LDS_AS bf16x8*)(lds + kb[ks & 1][sl >> 1] + (sl & 1) * SLOT + (ks >> 1) * 1024); };
  const int q4 = (lane >> 2) & 3, p4 = lane & 3;
  const int r7 = 4 * (g & 1) + q4;
  const int vrow = RING + (g >> 1) * 4096 + (wave >> 1) * 1024 + r7 * 128 + (p4 & 1) * 8;
  int vb[2][2];
  vb[0][0] = opaque(vrow + (((4 * (wave & 1) + (p4 >> 1)) ^ (r7 & 6)) * 16));
  vb[1][0] = opaque(vrow + (((4 * (wave & 1) + 2 + (p4 >> 1)) ^ (r7 & 6)) * 16));
  vb[0][1] = opaque(vb[0][0] + 2 * SLOT);
  vb[1][1] = opaque(vb[1][0] + 2 * SLOT);
  auto vread = [&](int sl, int j, int dt) {
    const int o = vb[dt][sl >> 1] + j * 16384;
    const s16x4 a0 = __builtin_amdgcn_ds_read_tr16_b64_v4i16((LDS_AS s16x4*)(lds + o));
    const s16x4 a1 = __builtin_amdgcn_ds_read_tr16_b64_v4i16((LDS_AS s16x4*)(lds + o + 8192));
    return __builtin_bit_cast(bf16x8, __builtin_shufflevector(a0, a1, 0, 1, 2, 3, 4, 5, 6, 7));
  };
  const int pw = opaque(P_OFF + (pair >> 1) * 5120 + RT0 * 1024 + lane * 16 + (pair & 1) * 8);
  const int pr = opaque(P_OFF + lane * 16);

  float mx[NT], mc[NT], ls[NT];
#pragma unroll
  for (int i = 0; i < NT; ++i) mx[i] = -INFINITY;
  f32x4 O[R][2];
  float inv[R];

  for (int pass = 0;; ++pass) {
  sync<8, 0>();
  STAMP(1);
  bf16x8 qf[NT][8];
  {
  const int l2 = lane_id(), r = l2 & 15, g = l2 >> 4;
#pragma unroll
  for (int ks = 0; ks < 8; ++ks) {
#pragma unroll
    for (int i = 0; i < NT; ++i) {
      const int rt = RT0 + i;
      const int rr = r;
      const int base = Q_OFF + (2 * rt + (rr >> 3)) * 4096 + (rr & 7) * 128;
      qf[i][ks] = *(LDS_AS bf16x8*)(lds + base + (ks >> 1) * 1024 + ((((ks & 1) * 4 + g) ^ (rr & 6)) * 16));
    }
  }
  }
    bf16x8 kf[8], vf[2][2], pf[2 * R];
    f32x4 sc[NT];
    f32x4 old[NT];
    float e[4];
#pragma unroll
    for (int i = 0; i < R; ++i) { O[i][0] = f32x4{0.f, 0.f, 0.f, 0.f}; O[i][1] = f32x4{0.f, 0.f, 0.f, 0.f}; }
#pragma unroll
    for (int i = 0; i < NT; ++i) ls[i] = 0.f;
#pragma unroll
    for (int ks = 0; ks < PD; ++ks) kf[ks] = kread(0, ks);
    auto pread = [&](int buf, int n) { return *(LDS_AS bf16x8*)(lds + pr + buf * PBUF + (n / R) * 5120 + (n % R) * 1024); };

    auto micro = [&](int m, const f32x4& s, int i, int buf) {
      if (m == 0) { e[0] = __builtin_amdgcn_exp2f(__builtin_fmaf(s[0], CEXP, mc[i])); e[1] = __builtin_fmaf(s[1], CEXP, mc[i]); }
      if (m == 1) { e[1] = __builtin_amdgcn_exp2f(e[1]); e[2] = __builtin_fmaf(s[2], CEXP, mc[i]); }
      if (m == 2) { e[2] = __builtin_amdgcn_exp2f(e[2]); e[3] = __builtin_fmaf(s[3], CEXP, mc[i]); }
      if (m == 3) { e[3] = __builtin_amdgcn_exp2f(e[3]); mx[i] = fmaxf(fmaxf(mx[i], s[0]), s[1]); }
      if (m == 4) { mx[i] = fmaxf(fmaxf(mx[i], s[2]), s[3]); ls[i] += (e[0] + e[1]) + (e[2] + e[3]); asm volatile("" : "+v"(ls[i]), "+v"(mx[i])); }
      if (m == 5) *(LDS_AS u32x2*)(lds + pw + buf * PBUF + i * 1024) = u32x2{cvtpk(e[0], e[1]), cvtpk(e[2], e[3])};
      SB();
    };
    auto stage_pv = [&](int sl, int buf, int part) {
      if (part == 0) pf[0] = pread(buf, 0);
      if (part == 1) { vf[0][0] = vread(sl, 0, 0); vf[0][1] = vread(sl, 0, 1); pf[1] = pread(buf, 1); }
      if (part == 2) { pf[2] = pread(buf, 2); pf[3] = pread(buf, 3); }
      if (part == 3 && R == 5) pf[R - 1] = pread(buf, R - 1);
    };
    auto qk_step = [&](auto SLC, auto MODEC, auto PBC, int u) {
      constexpr int SL = decltype(SLC)::value, MODE = decltype(MODEC)::value, PB = decltype(PBC)::value;
      constexpr int NS = (SL + 3) & 3;
      static_for<8>([&](auto KC) {
        constexpr int ks = decltype(KC)::value;
        if constexpr (MODE == 0 && ks == 8 - PD) { sync<4, 0>(); issue_tile<3>(cx, false, 2); SB(); }
        if constexpr (MODE == 1 && ks == 2) {
          sync<VS, 2>();
          const int o = REF_OFF + (RT0 * 16 + (lane_id() & 15)) * 16;
#pragma unroll
          for (int i = 0; i < NT; ++i) {
            const f32x4 m = *(LDS_AS f32x4*)(lds + o + i * 256);
            mc[i] = -fmaxf(fmaxf(m[0], m[1]), fmaxf(m[2], m[3])) * CEXP;
          }
        }
        if constexpr (MODE == 2 && ks == 4) sync<VS, 1>();
        if constexpr (ks + PD < 8) kf[ks + PD] = kread(SL, ks + PD);
        else if constexpr (MODE == 0) kf[ks + PD - 8] = kread(1, ks + PD - 8);
        if constexpr (MODE == 2 && ks >= 4) stage_pv((SL + 1) & 3, PB, ks - 4);
        SB();
#pragma unroll
        for (int i = 0; i < NT; ++i) {
          sc[i] = __builtin_amdgcn_mfma_f32_16x16x32_bf16(kf[ks], qf[i][ks], ks == 0 ? f32x4{0.f, 0.f, 0.f, 0.f} : sc[i], 0, 0, 0);
          SB();
          const int x = ks * NT + i;
          if (MODE == 2 && x < 6) micro(x, old[NT - 1], NT - 1, PB);
          if (MODE == 1 && ks >= 2) micro((x - 2 * NT) % 6, old[(x - 2 * NT) / 6], (x - 2 * NT) / 6, PB);
        }
        if constexpr (MODE == 2 && ks < 4) issue_at<HI, SL ^ 2, 5 + ks>(cx, false, u + 1);
        if constexpr (MODE != 0 && ks >= 4) issue_at<HI, NS, ks - 4>(cx, true, u);
      });
      if constexpr (MODE == 0) {
        const int xo = REF_OFF + (RT0 * 16 + (lane_id() & 15)) * 16 + pair * 4;
#pragma unroll
        for (int i = 0; i < NT; ++i) {
          float m = fmaxf(fmaxf(fmaxf(mx[i], sc[i][0]), sc[i][1]), fmaxf(sc[i][2], sc[i][3]));
          *(LDS_AS float*)(lds + xo + i * 256) = xg_max(m);
          old[i] = sc[i];
        }
        SB();
      }
      if constexpr (MODE == 1) {
        sync<63, 0>();
        stage_pv(2, 0, 0); stage_pv(2, 0, 1); stage_pv(2, 0, 2); stage_pv(2, 0, 3);
        SB();
      }
    };
    auto pv_step = [&](auto SLC, auto PBC, auto MODEC, int t) {
      constexpr int SL = decltype(SLC)::value, PB = decltype(PBC)::value, MODE = decltype(MODEC)::value;
      constexpr int NS = (SL + 3) & 3;
      static_for<2 * R>([&](auto NC) {
        constexpr int n = decltype(NC)::value;
        constexpr int j = n / R, i = n % R;
        if constexpr (MODE == 0 && n == R) sync<VS, 15>();
        if constexpr (MODE == 1 && n == 2 * R - 2) sync<0, 0>();
        if constexpr (n < 2) vf[1][n] = vread(SL, 1, n);
        if constexpr (n < R) pf[n + R] = pread(PB, n + R);
        if constexpr (MODE == 0 && n >= 2 * R - PD) kf[n - (2 * R - PD)] = kread((SL + 1) & 3, n - (2 * R - PD));
        if constexpr (MODE == 1 && n >= 2 * R - 2) { stage_pv(SL ^ 2, PB ^ 1, 2 * (n - 2 * R + 2)); stage_pv(SL ^ 2, PB ^ 1, 2 * (n - 2 * R + 2) + 1); }
        if constexpr (MODE == 2 && n == 2) {
          const int l = lane_id();
          t = __builtin_amdgcn_ballot_w64(*(LDS_AS unsigned*)(lds + FLG_OFF + (l & 7) * 4) != 0u) != 0ull;
#pragma unroll
          for (int q = 0; q < R; ++q) {
            const f32x4 sm = *(LDS_AS f32x4*)(lds + SUM_OFF + (l & 15) * 16 + q * 256);
            inv[q] = __builtin_amdgcn_rcpf((sm[0] + sm[1]) + (sm[2] + sm[3]));
          }
        } else SB();
#pragma unroll
        for (int h = 0; h < 2; ++h) {
          O[i][h] = __builtin_amdgcn_mfma_f32_16x16x32_bf16(vf[j][h], pf[n], O[i][h], 0, 0, 0);
          if (!(MODE == 2 && n >= 2 && n < 5)) SB();
          const int x = 2 * n + h;
          if (MODE == 0 && x >= 1 && x < 7) micro(x - 1, sc[0], 0, PB ^ 1);
          if (MODE == 0 && NT == 3 && x >= 10 && x < 16) micro(x - 10, sc[1], 1, PB ^ 1);
          if (MODE == 1 && x >= 1 && x <= 3 * NT) { micro((2 * x - 2) % 6, sc[(x - 1) / 3], (x - 1) / 3, PB ^ 1); micro((2 * x - 1) % 6, sc[(x - 1) / 3], (x - 1) / 3, PB ^ 1); }
          if (MODE == 1 && x > 3 * NT && x <= 4 * NT) {
            const int q = x - 3 * NT - 1;
            *(LDS_AS float*)(lds + SUM_OFF + (RT0 * 16 + (lane_id() & 15)) * 16 + pair * 4 + q * 256) = xg_sum(ls[q]);
            SB();
          }
          if (MODE == 1 && x == 4 * NT + 1) {
            bool bad = false;
#pragma unroll
            for (int q = 0; q < NT; ++q) bad |= !(__builtin_fmaf(mx[q], CEXP, mc[q]) <= THR);
            *(LDS_AS unsigned*)(lds + FLG_OFF + wave * 4) = __builtin_amdgcn_ballot_w64(bad) != 0ull;
            SB();
          }
        }
        if constexpr (MODE != 2 && n < R) issue_at<HI, SL ^ 2, 4 + n>(cx, true, t + 1);
        if constexpr (MODE == 0 && n >= R) issue_at<HI, NS, n - R>(cx, false, t + 3);
      });
      old[NT - 1] = sc[NT - 1];
      return t;
    };

    qk_step(IC(0), IC(0), IC(0), 0);
    qk_step(IC(1), IC(1), IC(0), 1);
    STAMP(2);
#pragma unroll 1
    for (int it = 0; it < 14; it += 2) {
      pv_step(IC(2), IC(0), IC(0), it);
      qk_step(IC(3), IC(2), IC(1), it + 2);
      pv_step(IC(0), IC(1), IC(0), it + 1);
      qk_step(IC(1), IC(2), IC(0), it + 3);
    }
    STAMP(3);
    pv_step(IC(2), IC(0), IC(1), 14);
    const int redo = pv_step(IC(0), IC(1), IC(2), 15);
    STAMP(4);
    if (!redo || pass == 1) break;
    sync<0, 0>();
    prologue<R>(cx, wave);
  }

  const int l3 = lane_id(), r3 = l3 & 15, g3 = l3 >> 4;
  __hip_bfloat16* op = outp + r3 * 256 + wave * 32 + (g3 & 1) * 16 + (g3 >> 1) * 8;
#pragma unroll
  for (int i = 0; i < R; ++i) {
    const unsigned a0 = cvtpk(O[i][0][0] * inv[i], O[i][0][1] * inv[i]), a1 = cvtpk(O[i][0][2] * inv[i], O[i][0][3] * inv[i]);
    const unsigned b0 = cvtpk(O[i][1][0] * inv[i], O[i][1][1] * inv[i]), b1 = cvtpk(O[i][1][2] * inv[i], O[i][1][3] * inv[i]);
    auto s0 = __builtin_amdgcn_permlane16_swap(a0, b0, false, false);
    auto s1 = __builtin_amdgcn_permlane16_swap(a1, b1, false, false);
    const u32x4 val = u32x4{s0[0], s1[0], s0[1], s1[1]};
    if (ONT) __builtin_nontemporal_store(val, (u32x4*)(op + i * 16 * 256));
    else *(u32x4*)(op + i * 16 * 256) = val;
  }
}

__global__ __launch_bounds__(512, 2) void gqa_attn_kernel(const __hip_bfloat16* __restrict__ q, const __hip_bfloat16* __restrict__ k,
                                                          const __hip_bfloat16* __restrict__ v, __hip_bfloat16* __restrict__ out) {
  __shared__ __attribute__((aligned(16))) char smem[LDS_BYTES];
  LDS_AS char* lds = (LDS_AS char*)smem;
  const int bid = blockIdx.x;
  const int idx = bid >> 3;
  const int grp = (bid & 7) * 8 + (idx >> 2);
  const int c = idx & 3;
  const int wave = __builtin_amdgcn_readfirstlane(threadIdx.x >> 6);
  const int lane = threadIdx.x & 63;

  Ctx cx;
  cx.kp = (void*)(k + (size_t)grp * 1024 * 256);
  cx.srdV = __builtin_amdgcn_make_buffer_rsrc((void*)(v + (size_t)grp * 1024 * 256), (short)0, 1024 * 512, 0x00020000);
  const size_t qrow0 = (size_t)grp * 288 + (c >> 1) * 144 + (c & 1) * 80;
  cx.srdQ = __builtin_amdgcn_make_buffer_rsrc((void*)(q + qrow0 * 256), (short)0, (c & 1) ? 64 * 512 : 80 * 512, 0x00020000);
  const unsigned ldsbase = (unsigned)(unsigned long long)lds;
  const unsigned lrow = (unsigned)(lane >> 3);
  const unsigned lsw = (unsigned)(((lane & 7) ^ (lrow & 6)) * 16) + lrow * 512;
  cx.voff = lsw + (unsigned)wave * 4096u;
  cx.ldsw = ldsbase + (unsigned)wave * 4096u;

  asm volatile("s_nop 4" ::: "memory");

  __hip_bfloat16* outp = out + qrow0 * 256;
  if (c & 1) {
    if (wave < 4) body<2, 0, 4>(lds, cx, wave, lane, outp);
    else body<2, 2, 4>(lds, cx, wave, lane, outp);
  } else {
    if (wave < 4) body<3, 0, 5>(lds, cx, wave, lane, outp);
    else body<2, 3, 5>(lds, cx, wave, lane, outp);
  }
}

}

extern "C" void kernel_launch(void* const* d_in, const int* in_sizes, int n_in, void* d_out, int out_size, void* d_ws,
                              size_t ws_size, hipStream_t stream) {
  const __hip_bfloat16* q = (const __hip_bfloat16*)d_in[0];
  const __hip_bfloat16* k = (const __hip_bfloat16*)d_in[1];
  const __hip_bfloat16* v = (const __hip_bfloat16*)d_in[2];
  gqa_attn_kernel<<<256, 512, 0, stream>>>(q, k, v, (__hip_bfloat16*)d_out);
}
```

```cpp
#include <hip/hip_runtime.h>
#include <hip/hip_bf16.h>
#include <utility>

typedef __bf16 bf16x8 __attribute__((ext_vector_type(8)));
typedef float f32x4 __attribute__((ext_vector_type(4)));
typedef float f32x2 __attribute__((ext_vector_type(2)));
typedef short s16x4 __attribute__((ext_vector_type(4)));
typedef unsigned u32x2 __attribute__((ext_vector_type(2)));
typedef unsigned u32x4 __attribute__((ext_vector_type(4)));
#define LDS_AS __attribute__((address_space(3)))

#define ROT 0
#define LDPOL " nt"
#define ONT 1
namespace {

constexpr int SLOT = 32768;
constexpr int PBUF = 10240;
constexpr int P_OFF = 0;
constexpr int REF_OFF = 2 * PBUF;
constexpr int SUM_OFF = REF_OFF + 1280;
constexpr int FLG_OFF = SUM_OFF + 1280;
constexpr int RING = 24576;
constexpr int Q_OFF = RING + 3 * SLOT;
constexpr int LDS_BYTES = Q_OFF + 40960;
#define SB() __builtin_amdgcn_sched_barrier(0)
#define IC(x) std::integral_constant<int, (x)>{}
template <int N, class F> __device__ __forceinline__ void static_for(F&& f) {
  [&]<int... I>(std::integer_sequence<int, I...>) { (f(IC(I)), ...); }(std::make_integer_sequence<int, N>{});
}

__device__ __forceinline__ unsigned cvtpk(float lo, float hi) {
  typedef __bf16 bf16x2 __attribute__((ext_vector_type(2)));
  return __builtin_bit_cast(unsigned, __builtin_convertvector(f32x2{lo, hi}, bf16x2));
}
template <int N> __device__ __forceinline__ void waitvm() { asm volatile("s_waitcnt vmcnt(%0)" ::"i"(N) : "memory"); }
__device__ __forceinline__ void wg_barrier() {
  asm volatile("s_waitcnt lgkmcnt(0)\n\ts_barrier" ::: "memory");
}
template <int OFF>
__device__ __forceinline__ void dma(__amdgpu_buffer_rsrc_t srd, unsigned voff, unsigned soff, unsigned m0v) {
  asm volatile("s_mov_b32 m0, %0\n\ts_nop 0\n\tbuffer_load_dwordx4 %1, %2, %3 offen offset:%4" LDPOL " lds"
               ::"s"(m0v), "v"(voff), "s"(srd), "s"(soff), "i"(OFF) : "memory");
}
__device__ __forceinline__ void dma_rowblock(__amdgpu_buffer_rsrc_t srd, unsigned voff, unsigned soff, unsigned ldsdst) {
  dma<0>(srd, voff, soff, ldsdst);
  dma<128>(srd, voff, soff, ldsdst + 896);
  dma<256>(srd, voff, soff, ldsdst + 2 * 896);
  dma<384>(srd, voff, soff, ldsdst + 3 * 896);
}
__device__ __forceinline__ float xg_max(float x) {
  auto s = __builtin_amdgcn_permlane16_swap(__float_as_uint(x), __float_as_uint(x), false, false);
  x = fmaxf(__uint_as_float(s[0]), __uint_as_float(s[1]));
  auto t = __builtin_amdgcn_permlane32_swap(__float_as_uint(x), __float_as_uint(x), false, false);
  return fmaxf(__uint_as_float(t[0]), __uint_as_float(t[1]));
}
__device__ __forceinline__ float xg_sum(float x) {
  auto s = __builtin_amdgcn_permlane16_swap(__float_as_uint(x), __float_as_uint(x), false, false);
  x = __uint_as_float(s[0]) + __uint_as_float(s[1]);
  auto t = __builtin_amdgcn_permlane32_swap(__float_as_uint(x), __float_as_uint(x), false, false);
  return __uint_as_float(t[0]) + __uint_as_float(t[1]);
}

#define STAMP(i)
struct Ctx {
  __amdgpu_buffer_rsrc_t srdQ;
  void *kp, *vp;
  unsigned voff;
  unsigned ldsw;
};

template <int SL, int PIECE> __device__ __forceinline__ void issue_piece(const Ctx& cx, bool isV, int tile) {
  const bool early = SL == 3 && !isV && tile == 16, skip = SL == 0 && isV && tile == 15;
  const __amdgpu_buffer_rsrc_t srd = __builtin_amdgcn_make_buffer_rsrc(isV || early ? cx.vp : cx.kp, (short)0, skip ? 0 : 1024 * 512, 0x00020000);
  dma<PIECE * 128>(srd, cx.voff, (unsigned)((early ? 15 : tile) * SLOT),
                   (unsigned)(RING + (early ? 0 : skip ? 3 : SL) * SLOT + PIECE * 896) + cx.ldsw);
}
template <bool HI, int SL, int G> __device__ __forceinline__ void issue_at(const Ctx& cx, bool isV, int tile) {
  constexpr int pos[2][4] = {{1, 3, 5, 7}, {0, 2, 6, 7}};
  static_for<4>([&](auto PC) {
    constexpr int P = decltype(PC)::value;
    if constexpr (pos[HI][P] == G) { issue_piece<SL, P>(cx, isV, tile); SB(); }
  });
}
template <int SL> __device__ __forceinline__ void issue_tile(const Ctx& cx, bool isV, int tile) {
  issue_piece<SL, 0>(cx, isV, tile); issue_piece<SL, 1>(cx, isV, tile); issue_piece<SL, 2>(cx, isV, tile); issue_piece<SL, 3>(cx, isV, tile);
}
template <int R> __device__ __forceinline__ void prologue(const Ctx& cx, int wave) {
  dma_rowblock(cx.srdQ, cx.voff, 0u, cx.ldsw + Q_OFF);
  if (R == 5) dma<0>(cx.srdQ, cx.voff, (unsigned)(32768 + (wave >> 2) * 4096 + (wave & 3) * 128 - wave * 4096), cx.ldsw + Q_OFF + 32768u - (unsigned)wave * 3072u);
  issue_tile<0>(cx, false, 0);
  issue_tile<1>(cx, false, 1);
  issue_tile<2>(cx, true, 0);
}
__device__ __forceinline__ int lane_id() {
  int l;
  asm volatile("v_mbcnt_lo_u32_b32 %0, -1, 0\n\tv_mbcnt_hi_u32_b32 %0, -1, %0" : "=v"(l));
  return l;
}
template <int VM, int LG> __device__ __forceinline__ void sync() {
  SB();
  asm volatile("s_waitcnt vmcnt(%0) lgkmcnt(%1)\n\ts_barrier" ::"i"(VM), "i"(LG) : "memory");
  SB();
}
__device__ __forceinline__ int opaque(int x) { asm volatile("" : "+v"(x)); return x; }

template <int NT, int RT0, int R>
__device__ __forceinline__ void body(LDS_AS char* lds, const Ctx& cx, int wave, int lane, __hip_bfloat16* __restrict__ outp) {
  const int pair = wave & 3;
  const int r = lane & 15, g = lane >> 4;
  constexpr float CEXP = 0.0625f * 1.4426950408889634f;
  constexpr float THR = 1048576.0f;
  constexpr int PD = 3;
  constexpr int VS = 4;
  constexpr bool HI = RT0 != 0;
  STAMP(0);
  prologue<R>(cx, wave);

  const int kbase = RING + (2 * pair + (r >> 3)) * 4096 + (r & 7) * 128;
  int kb[2][2];
  kb[0][0] = opaque(kbase + ((g ^ (r & 6)) * 16));
  kb[1][0] = opaque(kbase + (((4 + g) ^ (r & 6)) * 16));
  kb[0][1] = opaque(kb[0][0] + 2 * SLOT);
  kb[1][1] = opaque(kb[1][0] + 2 * SLOT);
  auto kread = [&](int sl, int ks) { return *(LDS_AS bf16x8*)(lds + kb[ks & 1][sl >> 1] + (sl & 1) * SLOT + (ks >> 1) * 1024); };
  const int q4 = (lane >> 2) & 3, p4 = lane & 3;
  const int r7 = 4 * (g & 1) + q4;
  const int vrow = RING + (g >> 1) * 4096 + (wave >> 1) * 1024 + r7 * 128 + (p4 & 1) * 8;
  int vb[2][2];
  vb[0][0] = opaque(vrow + (((4 * (wave & 1) + (p4 >> 1)) ^ (r7 & 6)) * 16));
  vb[1][0] = opaque(vrow + (((4 * (wave & 1) + 2 + (p4 >> 1)) ^ (r7 & 6)) * 16));
  vb[0][1] = opaque(vb[0][0] + 2 * SLOT);
  vb[1][1] = opaque(vb[1][0] + 2 * SLOT);
  auto vread = [&](int sl, int j, int dt) {
    const int o = vb[dt][sl >> 1] + j * 16384;
    const s16x4 a0 = __builtin_amdgcn_ds_read_tr16_b64_v4i16((LDS_AS s16x4*)(lds + o));
    const s16x4 a1 = __builtin_amdgcn_ds_read_tr16_b64_v4i16((LDS_AS s16x4*)(lds + o + 8192));
    return __builtin_bit_cast(bf16x8, __builtin_shufflevector(a0, a1, 0, 1, 2, 3, 4, 5, 6, 7));
  };
  const int pw = opaque(P_OFF + (pair >> 1) * 5120 + RT0 * 1024 + lane * 16 + (pair & 1) * 8);
  const int pr = opaque(P_OFF + lane * 16);

  float mx[NT], mc[NT];
  f32x2 ls[NT];
#pragma unroll
  for (int i = 0; i < NT; ++i) mx[i] = -INFINITY;

  for (int pass = 0;; ++pass) {
  sync<8, 0>();
  STAMP(1);
  bf16x8 qf[NT][8];
  {
  const int l2 = lane_id(), r = l2 & 15, g = l2 >> 4;
#pragma unroll
  for (int ks = 0; ks < 8; ++ks) {
#pragma unroll
    for (int i = 0; i < NT; ++i) {
      const int rt = RT0 + i;
      const int rr = r;
      const int base = Q_OFF + (2 * rt + (rr >> 3)) * 4096 + (rr & 7) * 128;
      qf[i][ks] = *(LDS_AS bf16x8*)(lds + base + (ks >> 1) * 1024 + ((((ks & 1) * 4 + g) ^ (rr & 6)) * 16));
    }
  }
  }
    f32x4 O[R][2];
    float inv[R];
    bf16x8 kf[8], vf[2][2], pf[2 * R];
    f32x4 sc[NT];
    f32x4 old[NT];
    f32x2 e0, e1;
    const bool track = pass == 1;
#pragma unroll
    for (int i = 0; i < R; ++i) { O[i][0] = f32x4{0.f, 0.f, 0.f, 0.f}; O[i][1] = f32x4{0.f, 0.f, 0.f, 0.f}; }
#pragma unroll
    for (int i = 0; i < NT; ++i) ls[i] = f32x2{0.f, 0.f};
#pragma unroll
    for (int ks = 0; ks < PD; ++ks) kf[ks] = kread(0, ks);
    auto pread = [&](int buf, int n) { return *(LDS_AS bf16x8*)(lds + pr + buf * PBUF + (n / R) * 5120 + (n % R) * 1024); };

    auto micro = [&](int m, const f32x4& s, int i, int buf) {
      const f32x2 cc = f32x2{CEXP, CEXP}, mm = f32x2{mc[i], mc[i]};
      if (m == 0) { e0 = __builtin_elementwise_fma(f32x2{s[0], s[1]}, cc, mm); e1 = __builtin_elementwise_fma(f32x2{s[2], s[3]}, cc, mm); }
      if (m == 1) { e0[0] = __builtin_amdgcn_exp2f(e0[0]); e0[1] = __builtin_amdgcn_exp2f(e0[1]); }
      if (m == 2) { e1[0] = __builtin_amdgcn_exp2f(e1[0]); e1[1] = __builtin_amdgcn_exp2f(e1[1]); }
      if (m == 3) { ls[i] += e0; ls[i] += e1; asm volatile("" : "+v"(ls[i])); }
      if (m == 4) *(LDS_AS u32x2*)(lds + pw + buf * PBUF + i * 1024) = u32x2{cvtpk(e0[0], e0[1]), cvtpk(e1[0], e1[1])};
      if (m == 5 && __builtin_expect(track, 0)) { mx[i] = fmaxf(fmaxf(mx[i], s[0]), s[1]); mx[i] = fmaxf(fmaxf(mx[i], s[2]), s[3]); asm volatile("" : "+v"(mx[i])); }
      SB();
    };
    auto stage_pv = [&](int sl, int buf, int part) {
      if (part == 0) pf[0] = pread(buf, 0);
      if (part == 1) { vf[0][0] = vread(sl, 0, 0); vf[0][1] = vread(sl, 0, 1); pf[1] = pread(buf, 1); }
      if (part == 2) { pf[2] = pread(buf, 2); pf[3] = pread(buf, 3); }
      if (part == 3 && R == 5) pf[R - 1] = pread(buf, R - 1);
    };
    auto qk_step = [&](auto SLC, auto MODEC, auto PBC, int u) {
      constexpr int SL = decltype(SLC)::value, MODE = decltype(MODEC)::value, PB = decltype(PBC)::value;
      constexpr int NS = (SL + 3) & 3;
      static_for<8>([&](auto KC) {
        constexpr int ks = decltype(KC)::value;
        if constexpr (MODE == 0 && ks == 8 - PD) { sync<4, 0>(); issue_tile<3>(cx, false, 2); SB(); }
        if constexpr (MODE == 1 && ks == 2) {
          sync<63, 2>();
          const int o = REF_OFF + (RT0 * 16 + (lane_id() & 15)) * 16;
#pragma unroll
          for (int i = 0; i < NT; ++i) {
            const f32x4 m = *(LDS_AS f32x4*)(lds + o + i * 256);
            mc[i] = -fmaxf(fmaxf(m[0], m[1]), fmaxf(m[2], m[3])) * CEXP;
          }
        }
        if constexpr (MODE == 2 && ks == 4) sync<VS, 1>();
        if constexpr (ks + PD < 8) kf[ks + PD] = kread(SL, ks + PD);
        else if constexpr (MODE == 0) kf[ks + PD - 8] = kread(1, ks + PD - 8);
        if constexpr (MODE == 2 && ks >= 4) stage_pv((SL + 1) & 3, PB, ks - 4);
        SB();
#pragma unroll
        for (int i = 0; i < NT; ++i) {
          sc[i] = __builtin_amdgcn_mfma_f32_16x16x32_bf16(kf[ks], qf[i][ks], ks == 0 ? f32x4{0.f, 0.f, 0.f, 0.f} : sc[i], 0, 0, 0);
          SB();
          const int x = ks * NT + i;
          if (MODE == 2 && x < 6) micro(x, old[NT - 1], NT - 1, PB);
          if (MODE == 1 && ks >= 2) micro((x - 2 * NT) % 6, old[(x - 2 * NT) / 6], (x - 2 * NT) / 6, PB);
        }
        if constexpr (MODE == 2 && ks < 4) issue_at<HI, SL ^ 2, 5 + ks>(cx, false, u + 1);
        if constexpr (MODE != 0 && ks >= 4) issue_at<HI, NS, ks - 4>(cx, true, u);
      });
      if constexpr (MODE == 0) {
        const int xo = REF_OFF + (RT0 * 16 + (lane_id() & 15)) * 16 + pair * 4;
#pragma unroll
        for (int i = 0; i < NT; ++i) {
          float m = fmaxf(fmaxf(fmaxf(mx[i], sc[i][0]), sc[i][1]), fmaxf(sc[i][2], sc[i][3]));
          *(LDS_AS float*)(lds + xo + i * 256) = xg_max(m);
          old[i] = sc[i];
        }
        SB();
      }
      if constexpr (MODE == 1) {
        sync<VS + 2, 0>();
        stage_pv(2, 0, 0); stage_pv(2, 0, 1); stage_pv(2, 0, 2); stage_pv(2, 0, 3);
        SB();
      }
    };
    int ow = 0;
    auto fin = [&](int i) {
      const unsigned a0 = cvtpk(O[i][0][0] * inv[i], O[i][0][1] * inv[i]), a1 = cvtpk(O[i][0][2] * inv[i], O[i][0][3] * inv[i]);
      const unsigned b0 = cvtpk(O[i][1][0] * inv[i], O[i][1][1] * inv[i]), b1 = cvtpk(O[i][1][2] * inv[i], O[i][1][3] * inv[i]);
      auto s0 = __builtin_amdgcn_permlane16_swap(a0, b0, false, false);
      auto s1 = __builtin_amdgcn_permlane16_swap(a1, b1, false, false);
      *(LDS_AS u32x4*)(lds + ow + i * 8192) = u32x4{s0[0], s1[0], s0[1], s1[1]};
      SB();
    };
    auto pv_step = [&](auto SLC, auto PBC, auto MODEC, int t) {
      constexpr int SL = decltype(SLC)::value, PB = decltype(PBC)::value, MODE = decltype(MODEC)::value;
      constexpr int NS = (SL + 3) & 3;
      static_for<2 * R>([&](auto NC) {
        constexpr int n = decltype(NC)::value;
        constexpr int j = n / R, i = n % R;
        if constexpr (MODE == 0 && n == R) sync<VS, R>();
        if constexpr (MODE == 1 && n == 2 * R - 2) sync<0, 0>();
        if constexpr (n < 2) vf[1][n] = vread(SL, 1, n);
        if constexpr (n < R) pf[n + R] = pread(PB, n + R);
        if constexpr (MODE == 0 && n >= 2 * R - PD) kf[n - (2 * R - PD)] = kread((SL + 1) & 3, n - (2 * R - PD));
        if constexpr (MODE == 1 && n >= 2 * R - 2) { stage_pv(SL ^ 2, PB ^ 1, 2 * (n - 2 * R + 2)); stage_pv(SL ^ 2, PB ^ 1, 2 * (n - 2 * R + 2) + 1); }
        if constexpr (MODE == 2 && n == 2) {
          const int l = lane_id();
          ow = RING + SLOT + (l & 15) * 512 + (((wave * 4 + ((l >> 4) & 1) * 2 + (l >> 5)) ^ (l & 7)) * 16);
          t = __builtin_amdgcn_ballot_w64(*(LDS_AS unsigned*)(lds + FLG_OFF + (l & 7) * 4) != 0u) != 0ull;
#pragma unroll
          for (int q = 0; q < R; ++q) {
            const f32x4 sm = *(LDS_AS f32x4*)(lds + SUM_OFF + (l & 15) * 16 + q * 256);
            inv[q] = __builtin_amdgcn_rcpf((sm[0] + sm[1]) + (sm[2] + sm[3]));
          }
        } else SB();
#pragma unroll
        for (int h = 0; h < 2; ++h) {
          O[i][h] = __builtin_amdgcn_mfma_f32_16x16x32_bf16(vf[j][h], pf[n], O[i][h], 0, 0, 0);
          if (!(MODE == 2 && n >= 2 && n < 5)) SB();
          const int x = 2 * n + h;
          if (MODE == 0 && x >= 1 && x < 7) micro(x - 1, sc[0], 0, PB ^ 1);
          if (MODE == 0 && NT == 3 && x >= 10 && x < 16) micro(x - 10, sc[1], 1, PB ^ 1);
          if (MODE == 1 && x >= 1 && x <= 3 * NT) { micro((2 * x - 2) % 6, sc[(x - 1) / 3], (x - 1) / 3, PB ^ 1); micro((2 * x - 1) % 6, sc[(x - 1) / 3], (x - 1) / 3, PB ^ 1); }
          if (MODE == 1 && x > 3 * NT && x <= 4 * NT) {
            const int q = x - 3 * NT - 1;
            *(LDS_AS float*)(lds + SUM_OFF + (RT0 * 16 + (lane_id() & 15)) * 16 + pair * 4 + q * 256) = xg_sum(ls[q][0] + ls[q][1]);
            SB();
          }
          if (MODE == 1 && x == 4 * NT + 1) {
            bool bad = false;
#pragma unroll
            for (int q = 0; q < NT; ++q) bad |= !(ls[q][0] + ls[q][1] <= THR);
            *(LDS_AS unsigned*)(lds + FLG_OFF + wave * 4) = __builtin_amdgcn_ballot_w64(bad) != 0ull;
            SB();
          }
        }
        if constexpr (MODE == 2 && n > R) fin(n - R - 1);
        if constexpr (MODE == 2 && n == 2 * R - 1) fin(R - 1);
        if constexpr (MODE == 0 && n < R) issue_at<HI, SL ^ 2, 4 + n>(cx, true, t + 1);
        if constexpr (MODE == 0 && n >= R) issue_at<HI, NS, n - R>(cx, false, t + 3);
      });
      old[NT - 1] = sc[NT - 1];
      return t;
    };

    qk_step(IC(0), IC(0), IC(0), 0);
    qk_step(IC(1), IC(1), IC(0), 1);
    STAMP(2);
#pragma unroll 1
    for (int it = 0; it < 14; it += 2) {
      pv_step(IC(2), IC(0), IC(0), it);
      qk_step(IC(3), IC(2), IC(1), it + 2);
      pv_step(IC(0), IC(1), IC(0), it + 1);
      qk_step(IC(1), IC(2), IC(0), it + 3);
    }
    STAMP(3);
    pv_step(IC(2), IC(0), IC(1), 14);
    const int redo = pv_step(IC(0), IC(1), IC(2), 15);
    STAMP(4);
    if (pass == 2 || (pass == 0 && !redo)) break;
    sync<0, 0>();
    prologue<R>(cx, wave);
  }

  const int l3 = lane_id();
  sync<63, 0>();
  const int row = 2 * wave + (l3 >> 5);
  const int orr = RING + SLOT + row * 512 + (((l3 & 31) ^ (row & 7)) * 16);
  __hip_bfloat16* op = outp + row * 256 + (l3 & 31) * 8;
#pragma unroll
  for (int i = 0; i < R; ++i) {
    const u32x4 val = *(LDS_AS u32x4*)(lds + orr + i * 8192);
    if (ONT) __builtin_nontemporal_store(val, (u32x4*)(op + i * 16 * 256));
    else *(u32x4*)(op + i * 16 * 256) = val;
  }
}

__global__ __launch_bounds__(512, 2) void gqa_attn_kernel(const __hip_bfloat16* __restrict__ q, const __hip_bfloat16* __restrict__ k,
                                                          const __hip_bfloat16* __restrict__ v, __hip_bfloat16* __restrict__ out) {
  __shared__ __attribute__((aligned(16))) char smem[LDS_BYTES];
  LDS_AS char* lds = (LDS_AS char*)smem;
  const int bid = blockIdx.x;
  const int idx = bid >> 3;
  const int grp = (bid & 7) * 8 + (idx >> 2);
  const int c = idx & 3;
  const int wave = __builtin_amdgcn_readfirstlane(threadIdx.x >> 6);
  const int lane = threadIdx.x & 63;

  Ctx cx;
  cx.kp = (void*)(k + (size_t)grp * 1024 * 256);
  cx.vp = (void*)(v + (size_t)grp * 1024 * 256);
  const size_t qrow0 = (size_t)grp * 288 + (c >> 1) * 144 + (c & 1) * 80;
  cx.srdQ = __builtin_amdgcn_make_buffer_rsrc((void*)(q + qrow0 * 256), (short)0, (c & 1) ? 64 * 512 : 80 * 512, 0x00020000);
  const unsigned ldsbase = (unsigned)(unsigned long long)lds;
  const unsigned lrow = (unsigned)(lane >> 3);
  const unsigned lsw = (unsigned)(((lane & 7) ^ (lrow & 6)) * 16) + lrow * 512;
  cx.voff = lsw + (unsigned)wave * 4096u;
  cx.ldsw = ldsbase + (unsigned)wave * 4096u;

  asm volatile("s_nop 4" ::: "memory");

  __hip_bfloat16* outp = out + qrow0 * 256;
  if (c & 1) {
    if (wave < 4) body<2, 0, 4>(lds, cx, wave, lane, outp);
    else body<2, 2, 4>(lds, cx, wave, lane, outp);
  } else {
    if (wave < 4) body<3, 0, 5>(lds, cx, wave, lane, outp);
    else body<2, 3, 5>(lds, cx, wave, lane, outp);
  }
}

}

extern "C" void kernel_launch(void* const* d_in, const int* in_sizes, int n_in, void* d_out, int out_size, void* d_ws,
                              size_t ws_size, hipStream_t stream) {
  const __hip_bfloat16* q = (const __hip_bfloat16*)d_in[0];
  const __hip_bfloat16* k = (const __hip_bfloat16*)d_in[1];
  const __hip_bfloat16* v = (const __hip_bfloat16*)d_in[2];
  gqa_attn_kernel<<<256, 512, 0, stream>>>(q, k, v, (__hip_bfloat16*)d_out);
}
```

```cpp
#include <hip/hip_runtime.h>
#include <hip/hip_bf16.h>
#include <utility>

typedef __bf16 bf16x8 __attribute__((ext_vector_type(8)));
typedef float f32x4 __attribute__((ext_vector_type(4)));
typedef float f32x2 __attribute__((ext_vector_type(2)));
typedef short s16x4 __attribute__((ext_vector_type(4)));
typedef unsigned u32x2 __attribute__((ext_vector_type(2)));
typedef unsigned u32x4 __attribute__((ext_vector_type(4)));
#define LDS_AS __attribute__((address_space(3)))

namespace {

constexpr int SLOT = 32768;
constexpr int PBUF = 10240;
constexpr int P_OFF = 0;
constexpr int REF_OFF = 2 * PBUF;
constexpr int SUM_OFF = REF_OFF + 1280;
constexpr int FLG_OFF = SUM_OFF + 1280;
constexpr int RING = 24576;
constexpr int Q_OFF = RING + 3 * SLOT;
constexpr int LDS_BYTES = Q_OFF + 40960;
#define SB() __builtin_amdgcn_sched_barrier(0)
#define IC(x) std::integral_constant<int, (x)>{}
template <int N, class F> __device__ __forceinline__ void static_for(F&& f) {
  [&]<int... I>(std::integer_sequence<int, I...>) { (f(IC(I)), ...); }(std::make_integer_sequence<int, N>{});
}

__device__ __forceinline__ unsigned cvtpk(float lo, float hi) {
  typedef __bf16 bf16x2 __attribute__((ext_vector_type(2)));
  return __builtin_bit_cast(unsigned, __builtin_convertvector(f32x2{lo, hi}, bf16x2));
}
template <int OFF>
__device__ __forceinline__ void dma(__amdgpu_buffer_rsrc_t srd, unsigned voff, unsigned soff, unsigned m0v) {
  asm volatile("s_mov_b32 m0, %0\n\ts_nop 0\n\tbuffer_load_dwordx4 %1, %2, %3 offen offset:%4 nt lds"
               ::"s"(m0v), "v"(voff), "s"(srd), "s"(soff), "i"(OFF) : "memory");
}
__device__ __forceinline__ void dma_rowblock(__amdgpu_buffer_rsrc_t srd, unsigned voff, unsigned soff, unsigned ldsdst) {
  dma<0>(srd, voff, soff, ldsdst);
  dma<128>(srd, voff, soff, ldsdst + 896);
  dma<256>(srd, voff, soff, ldsdst + 2 * 896);
  dma<384>(srd, voff, soff, ldsdst + 3 * 896);
}
__device__ __forceinline__ float xg_max(float x) {
  auto s = __builtin_amdgcn_permlane16_swap(__float_as_uint(x), __float_as_uint(x), false, false);
  x = fmaxf(__uint_as_float(s[0]), __uint_as_float(s[1]));
  auto t = __builtin_amdgcn_permlane32_swap(__float_as_uint(x), __float_as_uint(x), false, false);
  return fmaxf(__uint_as_float(t[0]), __uint_as_float(t[1]));
}
__device__ __forceinline__ float xg_sum(float x) {
  auto s = __builtin_amdgcn_permlane16_swap(__float_as_uint(x), __float_as_uint(x), false, false);
  x = __uint_as_float(s[0]) + __uint_as_float(s[1]);
  auto t = __builtin_amdgcn_permlane32_swap(__float_as_uint(x), __float_as_uint(x), false, false);
  return __uint_as_float(t[0]) + __uint_as_float(t[1]);
}

struct Ctx {
  __amdgpu_buffer_rsrc_t srdQ;
  void *kp, *vp;
  unsigned voff;
  unsigned ldsw;
};


template <int SL, int PIECE> __device__ __forceinline__ void issue_piece(const Ctx& cx, bool isV, int tile) {
  const bool early = SL == 3 && !isV && tile == 16, skip = SL == 0 && isV && tile == 15;
  const __amdgpu_buffer_rsrc_t srd = __builtin_amdgcn_make_buffer_rsrc(isV || early ? cx.vp : cx.kp, (short)0, skip ? 0 : 1024 * 512, 0x00020000);
  dma<PIECE * 128>(srd, cx.voff, (unsigned)((early ? 15 : tile) * SLOT),
                   (unsigned)(RING + (early ? 0 : skip ? 3 : SL) * SLOT + PIECE * 896) + cx.ldsw);
}
template <bool HI, int SL, int G> __device__ __forceinline__ void issue_at(const Ctx& cx, bool isV, int tile) {
  constexpr int pos[2][4] = {{1, 3, 5, 7}, {0, 2, 6, 7}};
  static_for<4>([&](auto PC) {
    constexpr int P = decltype(PC)::value;
    if constexpr (pos[HI][P] == G) { issue_piece<SL, P>(cx, isV, tile); SB(); }
  });
}
template <int SL> __device__ __forceinline__ void issue_tile(const Ctx& cx, bool isV, int tile) {
  issue_piece<SL, 0>(cx, isV, tile); issue_piece<SL, 1>(cx, isV, tile); issue_piece<SL, 2>(cx, isV, tile); issue_piece<SL, 3>(cx, isV, tile);
}
template <int R> __device__ __forceinline__ void prologue(const Ctx& cx, int wave) {
  dma_rowblock(cx.srdQ, cx.voff, 0u, cx.ldsw + Q_OFF);
  if (R == 5) dma<0>(cx.srdQ, cx.voff, (unsigned)(32768 + (wave >> 2) * 4096 + (wave & 3) * 128 - wave * 4096), cx.ldsw + Q_OFF + 32768u - (unsigned)wave * 3072u);
  issue_tile<0>(cx, false, 0);
  issue_tile<1>(cx, false, 1);
  issue_tile<2>(cx, true, 0);
}
__device__ __forceinline__ int lane_id() {
  int l;
  asm volatile("v_mbcnt_lo_u32_b32 %0, -1, 0\n\tv_mbcnt_hi_u32_b32 %0, -1, %0" : "=v"(l));
  return l;
}
template <int VM, int LG> __device__ __forceinline__ void sync() {
  SB();
  asm volatile("s_waitcnt vmcnt(%0) lgkmcnt(%1)\n\ts_barrier" ::"i"(VM), "i"(LG) : "memory");
  SB();
}
__device__ __forceinline__ int opaque(int x) { asm volatile("" : "+v"(x)); return x; }

template <int NT, int RT0, int R>
__device__ __forceinline__ void body(LDS_AS char* lds, const Ctx& cx, int wave, int lane, __hip_bfloat16* __restrict__ outp) {
  const int pair = wave & 3;
  const int r = lane & 15, g = lane >> 4;
  constexpr float CEXP = 0.0625f * 1.4426950408889634f;
  constexpr float THR = 1048576.0f;
  constexpr int PD = 3;
  constexpr int VS = 4;
  constexpr bool HI = RT0 != 0;
  prologue<R>(cx, wave);

  const int kbase = RING + (2 * pair + (r >> 3)) * 4096 + (r & 7) * 128;
  int kb[2][2];
  kb[0][0] = opaque(kbase + ((g ^ (r & 6)) * 16));
  kb[1][0] = opaque(kbase + (((4 + g) ^ (r & 6)) * 16));
  kb[0][1] = opaque(kb[0][0] + 2 * SLOT);
  kb[1][1] = opaque(kb[1][0] + 2 * SLOT);
  auto kread = [&](int sl, int ks) { return *(LDS_AS bf16x8*)(lds + kb[ks & 1][sl >> 1] + (sl & 1) * SLOT + (ks >> 1) * 1024); };
  const int q4 = (lane >> 2) & 3, p4 = lane & 3;
  const int r7 = 4 * (g & 1) + q4;
  const int vrow = RING + (g >> 1) * 4096 + (wave >> 1) * 1024 + r7 * 128 + (p4 & 1) * 8;
  int vb[2][2];
  vb[0][0] = opaque(vrow + (((4 * (wave & 1) + (p4 >> 1)) ^ (r7 & 6)) * 16));
  vb[1][0] = opaque(vrow + (((4 * (wave & 1) + 2 + (p4 >> 1)) ^ (r7 & 6)) * 16));
  vb[0][1] = opaque(vb[0][0] + 2 * SLOT);
  vb[1][1] = opaque(vb[1][0] + 2 * SLOT);
  auto vread = [&](int sl, int j, int dt) {
    const int o = vb[dt][sl >> 1] + j * 16384;
    const s16x4 a0 = __builtin_amdgcn_ds_read_tr16_b64_v4i16((LDS_AS s16x4*)(lds + o));
    const s16x4 a1 = __builtin_amdgcn_ds_read_tr16_b64_v4i16((LDS_AS s16x4*)(lds + o + 8192));
    return __builtin_bit_cast(bf16x8, __builtin_shufflevector(a0, a1, 0, 1, 2, 3, 4, 5, 6, 7));
  };
  const int pw = opaque(P_OFF + (pair >> 1) * 5120 + RT0 * 1024 + lane * 16 + (pair & 1) * 8);
  const int pr = opaque(P_OFF + lane * 16);

  float mx[NT], mc[NT];
  f32x2 ls[NT];
#pragma unroll
  for (int i = 0; i < NT; ++i) mx[i] = -INFINITY;

  for (int pass = 0;; ++pass) {
  sync<8, 0>();
  bf16x8 qf[NT][8];
  {
  const int l2 = lane_id(), r = l2 & 15, g = l2 >> 4;
#pragma unroll
  for (int ks = 0; ks < 8; ++ks) {
#pragma unroll
    for (int i = 0; i < NT; ++i) {
      const int rt = RT0 + i;
      const int rr = r;
      const int base = Q_OFF + (2 * rt + (rr >> 3)) * 4096 + (rr & 7) * 128;
      qf[i][ks] = *(LDS_AS bf16x8*)(lds + base + (ks >> 1) * 1024 + ((((ks & 1) * 4 + g) ^ (rr & 6)) * 16));
    }
  }
  }
    f32x4 O[R][2];
    float inv[R];
    bf16x8 kf[8], vf[2][2], pf[2 * R];
    f32x4 sc[NT];
    f32x4 old[NT];
    f32x2 e0, e1;
    const bool track = pass == 1;
#pragma unroll
    for (int i = 0; i < R; ++i) { O[i][0] = f32x4{0.f, 0.f, 0.f, 0.f}; O[i][1] = f32x4{0.f, 0.f, 0.f, 0.f}; }
#pragma unroll
    for (int i = 0; i < NT; ++i) ls[i] = f32x2{0.f, 0.f};
#pragma unroll
    for (int ks = 0; ks < PD; ++ks) kf[ks] = kread(0, ks);
    auto pread = [&](int buf, int n) { return *(LDS_AS bf16x8*)(lds + pr + buf * PBUF + (n / R) * 5120 + (n % R) * 1024); };

    auto micro = [&](int m, const f32x4& s, int i, int buf) {
      const f32x2 cc = f32x2{CEXP, CEXP}, mm = f32x2{mc[i], mc[i]};
      if (m == 0) { e0 = __builtin_elementwise_fma(f32x2{s[0], s[1]}, cc, mm); e1 = __builtin_elementwise_fma(f32x2{s[2], s[3]}, cc, mm); }
      if (m == 1) { e0[0] = __builtin_amdgcn_exp2f(e0[0]); e0[1] = __builtin_amdgcn_exp2f(e0[1]); }
      if (m == 2) { e1[0] = __builtin_amdgcn_exp2f(e1[0]); e1[1] = __builtin_amdgcn_exp2f(e1[1]); }
      if (m == 3) { ls[i] += e0; ls[i] += e1; asm volatile("" : "+v"(ls[i])); }
      if (m == 4) *(LDS_AS u32x2*)(lds + pw + buf * PBUF + i * 1024) = u32x2{cvtpk(e0[0], e0[1]), cvtpk(e1[0], e1[1])};
      if (m == 5 && __builtin_expect(track, 0)) { mx[i] = fmaxf(fmaxf(mx[i], s[0]), s[1]); mx[i] = fmaxf(fmaxf(mx[i], s[2]), s[3]); asm volatile("" : "+v"(mx[i])); }
      SB();
    };
    auto stage_pv = [&](int sl, int buf, int part) {
      if (part == 0) pf[0] = pread(buf, 0);
      if (part == 1) { vf[0][0] = vread(sl, 0, 0); vf[0][1] = vread(sl, 0, 1); pf[1] = pread(buf, 1); }
      if (part == 2) { pf[2] = pread(buf, 2); pf[3] = pread(buf, 3); }
      if (part == 3 && R == 5) pf[R - 1] = pread(buf, R - 1);
    };
    auto qk_step = [&](auto SLC, auto MODEC, auto PBC, int u) {
      constexpr int SL = decltype(SLC)::value, MODE = decltype(MODEC)::value, PB = decltype(PBC)::value;
      constexpr int NS = (SL + 3) & 3;
      static_for<8>([&](auto KC) {
        constexpr int ks = decltype(KC)::value;
        if constexpr (MODE == 0 && ks == 8 - PD) { sync<4, 0>(); issue_tile<3>(cx, false, 2); SB(); }
        if constexpr (MODE == 1 && ks == 2) {
          sync<63, 2>();
          const int o = REF_OFF + (RT0 * 16 + (lane_id() & 15)) * 16;
#pragma unroll
          for (int i = 0; i < NT; ++i) {
            const f32x4 m = *(LDS_AS f32x4*)(lds + o + i * 256);
            mc[i] = -fmaxf(fmaxf(m[0], m[1]), fmaxf(m[2], m[3])) * CEXP;
          }
        }
        if constexpr (MODE == 2 && ks == 4) sync<VS, 1>();
        if constexpr (ks + PD < 8) kf[ks + PD] = kread(SL, ks + PD);
        else if constexpr (MODE == 0) kf[ks + PD - 8] = kread(1, ks + PD - 8);
        if constexpr (MODE == 2 && ks >= 4) stage_pv((SL + 1) & 3, PB, ks - 4);
        SB();
#pragma unroll
        for (int i = 0; i < NT; ++i) {
          sc[i] = __builtin_amdgcn_mfma_f32_16x16x32_bf16(kf[ks], qf[i][ks], ks == 0 ? f32x4{0.f, 0.f, 0.f, 0.f} : sc[i], 0, 0, 0);
          SB();
          const int x = ks * NT + i;
          if (MODE == 2 && x < 6) micro(x, old[NT - 1], NT - 1, PB);
          if (MODE == 1 && ks >= 2) micro((x - 2 * NT) % 6, old[(x - 2 * NT) / 6], (x - 2 * NT) / 6, PB);
        }
        if constexpr (MODE == 2 && ks < 4) issue_at<HI, SL ^ 2, 5 + ks>(cx, false, u + 1);
        if constexpr (MODE != 0 && ks >= 4) issue_at<HI, NS, ks - 4>(cx, true, u);
      });
      if constexpr (MODE == 0) {
        const int xo = REF_OFF + (RT0 * 16 + (lane_id() & 15)) * 16 + pair * 4;
#pragma unroll
        for (int i = 0; i < NT; ++i) {
          float m = fmaxf(fmaxf(fmaxf(mx[i], sc[i][0]), sc[i][1]), fmaxf(sc[i][2], sc[i][3]));
          *(LDS_AS float*)(lds + xo + i * 256) = xg_max(m);
          old[i] = sc[i];
        }
        SB();
      }
      if constexpr (MODE == 1) {
        sync<VS + 2, 0>();
        stage_pv(2, 0, 0); stage_pv(2, 0, 1); stage_pv(2, 0, 2); stage_pv(2, 0, 3);
        SB();
      }
    };
    int ow = 0;
    auto fin = [&](int i) {
      const unsigned a0 = cvtpk(O[i][0][0] * inv[i], O[i][0][1] * inv[i]), a1 = cvtpk(O[i][0][2] * inv[i], O[i][0][3] * inv[i]);
      const unsigned b0 = cvtpk(O[i][1][0] * inv[i], O[i][1][1] * inv[i]), b1 = cvtpk(O[i][1][2] * inv[i], O[i][1][3] * inv[i]);
      auto s0 = __builtin_amdgcn_permlane16_swap(a0, b0, false, false);
      auto s1 = __builtin_amdgcn_permlane16_swap(a1, b1, false, false);
      *(LDS_AS u32x4*)(lds + ow + i * 8192) = u32x4{s0[0], s1[0], s0[1], s1[1]};
      SB();
    };
    auto pv_step = [&](auto SLC, auto PBC, auto MODEC, int t) {
      constexpr int SL = decltype(SLC)::value, PB = decltype(PBC)::value, MODE = decltype(MODEC)::value;
      constexpr int NS = (SL + 3) & 3;
      unsigned fl = 0;
      f32x4 sm[R];
      int so = 0;
      static_for<2 * R>([&](auto NC) {
        constexpr int n = decltype(NC)::value;
        constexpr int j = n / R, i = n % R;
        if constexpr (MODE == 0 && n == R) sync<VS, R>();
        if constexpr (MODE == 1 && n == 2 * R - 2) sync<0, 0>();
        if constexpr (n < 2) vf[1][n] = vread(SL, 1, n);
        if constexpr (n < R) pf[n + R] = pread(PB, n + R);
        if constexpr (MODE == 0 && n >= 2 * R - PD) kf[n - (2 * R - PD)] = kread((SL + 1) & 3, n - (2 * R - PD));
        if constexpr (MODE == 1 && n >= 2 * R - 2) { stage_pv(SL ^ 2, PB ^ 1, 2 * (n - 2 * R + 2)); stage_pv(SL ^ 2, PB ^ 1, 2 * (n - 2 * R + 2) + 1); }
        if constexpr (MODE == 2 && n == 0) {
          const int l = lane_id();
          ow = RING + SLOT + (l & 15) * 512 + (((wave * 4 + ((l >> 4) & 1) * 2 + (l >> 5)) ^ (l & 7)) * 16);
          fl = *(LDS_AS unsigned*)(lds + FLG_OFF + (l & 7) * 4);
#pragma unroll
          for (int q = 0; q < R; ++q) sm[q] = *(LDS_AS f32x4*)(lds + SUM_OFF + (l & 15) * 16 + q * 256);
        }
        SB();
#pragma unroll
        for (int h = 0; h < 2; ++h) {
          O[i][h] = __builtin_amdgcn_mfma_f32_16x16x32_bf16(vf[j][h], pf[n], O[i][h], 0, 0, 0);
          SB();
          const int x = 2 * n + h;
          if (MODE == 2 && x == 3) { t = __builtin_amdgcn_ballot_w64(fl != 0u) != 0ull; SB(); }
          if (MODE == 2 && x >= 4 && x < 4 + R) { inv[x - 4] = __builtin_amdgcn_rcpf(((sm[x - 4][0] + sm[x - 4][1]) + sm[x - 4][2]) + sm[x - 4][3]); SB(); }
          if (MODE == 0 && x >= 1 && x < 7) micro(x - 1, sc[0], 0, PB ^ 1);
          if (MODE == 0 && NT == 3 && x >= 10 && x < 16) micro(x - 10, sc[1], 1, PB ^ 1);
          if (MODE == 1 && x >= 1 && x <= 3 * NT) { micro((2 * x - 2) % 6, sc[(x - 1) / 3], (x - 1) / 3, PB ^ 1); micro((2 * x - 1) % 6, sc[(x - 1) / 3], (x - 1) / 3, PB ^ 1); }
          if (MODE == 1 && x > 3 * NT && x <= 4 * NT) {
            const int q = x - 3 * NT - 1;
            if (q == 0) so = SUM_OFF + (RT0 * 16 + (lane_id() & 15)) * 16 + pair * 4;
            *(LDS_AS float*)(lds + so + q * 256) = xg_sum(ls[q][0] + ls[q][1]);
            SB();
          }
          if (MODE == 1 && x == 4 * NT + 1) {
            bool bad = false;
#pragma unroll
            for (int q = 0; q < NT; ++q) bad |= !(ls[q][0] + ls[q][1] <= THR);
            *(LDS_AS unsigned*)(lds + FLG_OFF + wave * 4) = __builtin_amdgcn_ballot_w64(bad) != 0ull;
            SB();
          }
        }
        if constexpr (MODE == 2 && n > R) fin(n - R - 1);
        if constexpr (MODE == 2 && n == 2 * R - 1) fin(R - 1);
        if constexpr (MODE == 0 && n < R) issue_at<HI, SL ^ 2, 4 + n>(cx, true, t + 1);
        if constexpr (MODE == 0 && n >= R) issue_at<HI, NS, n - R>(cx, false, t + 3);
      });
      old[NT - 1] = sc[NT - 1];
      return t;
    };

    qk_step(IC(0), IC(0), IC(0), 0);
    qk_step(IC(1), IC(1), IC(0), 1);
#pragma unroll 1
    for (int it = 0; it < 14; it += 2) {
      pv_step(IC(2), IC(0), IC(0), it);
      qk_step(IC(3), IC(2), IC(1), it + 2);
      pv_step(IC(0), IC(1), IC(0), it + 1);
      qk_step(IC(1), IC(2), IC(0), it + 3);
    }
    pv_step(IC(2), IC(0), IC(1), 14);
    const int redo = pv_step(IC(0), IC(1), IC(2), 15);
    if (pass == 2 || (pass == 0 && !redo)) break;
    sync<0, 0>();
    prologue<R>(cx, wave);
  }

  const int l3 = lane_id();
  sync<63, 0>();
  const int row = 2 * wave + (l3 >> 5);
  const int orr = RING + SLOT + row * 512 + (((l3 & 31) ^ (row & 7)) * 16);
  __hip_bfloat16* op = outp + row * 256 + (l3 & 31) * 8;
#pragma unroll
  for (int i = 0; i < R; ++i) {
    const u32x4 val = *(LDS_AS u32x4*)(lds + orr + i * 8192);
    __builtin_nontemporal_store(val, (u32x4*)(op + i * 16 * 256));
  }
}

__global__ __launch_bounds__(512, 2) void gqa_attn_kernel(const __hip_bfloat16* __restrict__ q, const __hip_bfloat16* __restrict__ k,
                                                          const __hip_bfloat16* __restrict__ v, __hip_bfloat16* __restrict__ out) {
  __shared__ __attribute__((aligned(16))) char smem[LDS_BYTES];
  LDS_AS char* lds = (LDS_AS char*)smem;
  const int bid = blockIdx.x;
  const int idx = bid >> 3;
  const int grp = (bid & 7) * 8 + (idx >> 2);
  const int c = idx & 3;
  const int wave = __builtin_amdgcn_readfirstlane(threadIdx.x >> 6);
  const int lane = threadIdx.x & 63;

  Ctx cx;
  cx.kp = (void*)(k + (size_t)grp * 1024 * 256);
  cx.vp = (void*)(v + (size_t)grp * 1024 * 256);
  const size_t qrow0 = (size_t)grp * 288 + (c >> 1) * 144 + (c & 1) * 80;
  cx.srdQ = __builtin_amdgcn_make_buffer_rsrc((void*)(q + qrow0 * 256), (short)0, (c & 1) ? 64 * 512 : 80 * 512, 0x00020000);
  const unsigned ldsbase = (unsigned)(unsigned long long)lds;
  const unsigned lrow = (unsigned)(lane >> 3);
  const unsigned lsw = (unsigned)(((lane & 7) ^ (lrow & 6)) * 16) + lrow * 512;
  cx.voff = lsw + (unsigned)wave * 4096u;
  cx.ldsw = ldsbase + (unsigned)wave * 4096u;

  asm volatile("s_nop 4" ::: "memory");

  __hip_bfloat16* outp = out + qrow0 * 256;
  if (c & 1) {
    if (wave < 4) body<2, 0, 4>(lds, cx, wave, lane, outp);
    else body<2, 2, 4>(lds, cx, wave, lane, outp);
  } else {
    if (wave < 4) body<3, 0, 5>(lds, cx, wave, lane, outp);
    else body<2, 3, 5>(lds, cx, wave, lane, outp);
  }
}

}

extern "C" void kernel_launch(void* const* d_in, const int* in_sizes, int n_in, void* d_out, int out_size, void* d_ws,
                              size_t ws_size, hipStream_t stream) {
  const __hip_bfloat16* q = (const __hip_bfloat16*)d_in[0];
  const __hip_bfloat16* k = (const __hip_bfloat16*)d_in[1];
  const __hip_bfloat16* v = (const __hip_bfloat16*)d_in[2];
  gqa_attn_kernel<<<256, 512, 0, stream>>>(q, k, v, (__hip_bfloat16*)d_out);
}
```
